```python
import torch

def _softmax(x, axis=-1):
    return torch.softmax(x, dim=axis)

def _gelu(x):
    return torch.nn.functional.gelu(x, approximate='tanh')

def _silu(x):
    return torch.nn.functional.silu(x)

def _sigmoid(x):
    return torch.sigmoid(x)

def _layernorm(x, axis=-1, eps=1e-05):
    m = x.mean(dim=axis, keepdim=True)
    v = x.var(dim=axis, keepdim=True, unbiased=False)
    return (x - m) / torch.sqrt(v + eps)

def _scatter_add(tgt, idx, src, axis=0):
    return tgt.clone().index_add_(axis, idx, src)

def _segment_sum(src, seg, n):
    out = torch.zeros((n,) + src.shape[1:], dtype=src.dtype)
    return out.index_add_(0, seg, src)

def _pad(x, pw):
    flat = tuple((v for pair in reversed(pw) for v in pair))
    return torch.nn.functional.pad(x, flat)
kernel_name = 'adaLN_cond__spectral_fnet__geglu_ffn__prompt'

def setup_inputs():

    _w = lambda a: (lambda t: t.to(torch.bfloat16) if t.is_floating_point() else t)(torch.as_tensor(a))

    def _A_setup():
        B, S, D = (1, 2048, 1024)
        ns = torch.arange(S)[:, None] * torch.arange(S)[None, :]
        nd = torch.arange(D)[:, None] * torch.arange(D)[None, :]
        Ws_re = torch.cos(-2.0 * torch.pi * ns / S).to(float)
        Ws_im = torch.sin(-2.0 * torch.pi * ns / S).to(float)
        Wd_re = torch.cos(-2.0 * torch.pi * nd / D).to(float)
        Wd_im = torch.sin(-2.0 * torch.pi * nd / D).to(float)
        return {'x': _w((lambda *s: torch.randn(*s, dtype=torch.bfloat16) * 0.05)(B, S, D)), 'dft_seq_re': _w(Ws_re), 'dft_seq_im': _w(Ws_im), 'dft_hid_re': _w(Wd_re), 'dft_hid_im': _w(Wd_im)}
    _A_in = _A_setup()

    def _B_setup():
        B, S, D, F = (1, 2048, 1024, 3584)
        return {'x': _w((lambda *s: torch.randn(*s, dtype=torch.bfloat16) * 0.05)(B, S, D)), 'w_gate': _w((lambda *s: torch.randn(*s, dtype=torch.bfloat16) * 0.4)(D, F)), 'w_up': _w((lambda *s: torch.randn(*s, dtype=torch.bfloat16) * 0.4)(D, F)), 'w_down': _w((lambda *s: torch.randn(*s, dtype=torch.bfloat16) * 0.4)(F, D))}
    _B_in = _B_setup()
    return {'A_x': _A_in['x'], 'A_dft_seq_re': _A_in['dft_seq_re'], 'A_dft_seq_im': _A_in['dft_seq_im'], 'A_dft_hid_re': _A_in['dft_hid_re'], 'A_dft_hid_im': _A_in['dft_hid_im'], 'B_x': _B_in['x'], 'B_w_gate': _B_in['w_gate'], 'B_w_up': _B_in['w_up'], 'B_w_down': _B_in['w_down'], 'c': _w((lambda *s: torch.randn(*s, dtype=torch.bfloat16) * 0.1)(1, 1024) * 8.0), 'w_mod': _w((lambda *s: torch.randn(*s, dtype=torch.bfloat16) * 0.1)(1024, 6 * 1024) * 1024 ** (-0.5) * 8.0)}

def reference(A_x, A_dft_seq_re, A_dft_seq_im, A_dft_hid_re, A_dft_hid_im, B_x, B_w_gate, B_w_up, B_w_down, c, w_mod):

    def _A_ref(x, dft_seq_re, dft_seq_im, dft_hid_re, dft_hid_im):
        a = torch.einsum('bsd,ed->bse', x, dft_hid_re)
        b = torch.einsum('bsd,ed->bse', x, dft_hid_im)
        y = torch.einsum('ts,bse->bte', dft_seq_re, a) - torch.einsum('ts,bse->bte', dft_seq_im, b)
        return y

    def _B_ref(x, w_gate, w_up, w_down):
        return _gelu(x @ w_gate) * (x @ w_up) @ w_down
    x = A_x
    mod = (_silu(c) @ w_mod).reshape(c.shape[0], 6, 1, -1)
    sa, ca, ga, sb, cb, gb = (mod[:, 0].clone(), mod[:, 1].clone(), mod[:, 2].clone(), mod[:, 3].clone(), mod[:, 4].clone(), mod[:, 5].clone())
    h = _layernorm(x, axis=-1) * (1 + ca) + sa
    x = x + ga * _A_ref(h, A_dft_seq_re, A_dft_seq_im, A_dft_hid_re, A_dft_hid_im)
    h = _layernorm(x, axis=-1) * (1 + cb) + sb
    x = x + gb * _B_ref(h, B_w_gate, B_w_up, B_w_down)
    return x
```

```cpp
#include <hip/hip_runtime.h>
#include <hip/hip_bf16.h>

typedef unsigned short u16;
typedef unsigned int u32;
typedef __bf16 bf16x8 __attribute__((ext_vector_type(8)));
typedef __bf16 bf16x2 __attribute__((ext_vector_type(2)));
typedef short s16x4 __attribute__((ext_vector_type(4)));
typedef float f32x4 __attribute__((ext_vector_type(4)));
typedef float f32x2 __attribute__((ext_vector_type(2)));
typedef u32 u32x4 __attribute__((ext_vector_type(4)));
typedef u32 u32x2 __attribute__((ext_vector_type(2)));
#define LDS_AS __attribute__((address_space(3)))
#define DI __device__ __forceinline__

constexpr int S_ = 2048, D_ = 1024, F_ = 3584;
constexpr int KS = 4;

constexpr size_t WS_MODP = 0;
constexpr size_t WS_RS = 131072;
constexpr size_t WS_NT = 1u << 20;
constexpr size_t WS_PP = WS_NT + (4u << 20);
constexpr size_t WS_X2 = WS_PP + (8u << 20);
constexpr size_t WS_H2 = WS_X2 + (4u << 20);
constexpr size_t WS_P = WS_H2 + (4u << 20);

DI float bf_lo(u32 v) { return __builtin_bit_cast(float, v << 16); }
DI float bf_hi(u32 v) { return __builtin_bit_cast(float, v & 0xffff0000u); }
DI u32 pack2(float a, float b) {
  f32x2 v = {a, b};
  return __builtin_bit_cast(u32, __builtin_convertvector(v, bf16x2));
}
DI u32 f2u(float a) { return __builtin_bit_cast(u32, a); }
DI float u2f(u32 a) { return __builtin_bit_cast(float, a); }
DI float rbf(float a) { return bf_lo(pack2(a, a)); }
DI float wave_sum(float v) {
#pragma unroll
  for (int o = 32; o; o >>= 1) v += __shfl_xor(v, o, 64);
  return v;
}
DI float modval(const float* modp, int j) {
  float s = 0.f;
#pragma unroll
  for (int k = 0; k < KS; k++) s += modp[k * 6144 + j];
  return rbf(s);
}
DI f32x4 mfma16(bf16x8 a, bf16x8 b, f32x4 c) { return __builtin_amdgcn_mfma_f32_16x16x32_bf16(a, b, c, 0, 0, 0); }
DI bf16x8 lds_b128(u32 addr) { return *(const LDS_AS bf16x8*)(size_t)addr; }
DI bf16x8 lds_tr2(u32 a0, u32 a1) {
  s16x4 lo = __builtin_amdgcn_ds_read_tr16_b64_v4i16((LDS_AS s16x4*)(size_t)a0);
  s16x4 hi = __builtin_amdgcn_ds_read_tr16_b64_v4i16((LDS_AS s16x4*)(size_t)a1);
  u32x2 l = __builtin_bit_cast(u32x2, lo), h = __builtin_bit_cast(u32x2, hi);
  u32x4 r = {l[0], l[1], h[0], h[1]};
  return __builtin_bit_cast(bf16x8, r);
}
#define WAIT_V(n) asm volatile("s_waitcnt vmcnt(%0)" ::"i"(n) : "memory")
#define BAR() asm volatile("s_barrier" ::: "memory")
#define LBAR() asm volatile("s_waitcnt lgkmcnt(0)\n\ts_barrier" ::: "memory")

DI void glds4(const char* sbase, u32 m0v, u32 v0, u32 v1, u32 v2, u32 v3) {
  asm volatile(
      "s_mov_b32 m0, %1\n\ts_nop 0\n\t"
      "global_load_lds_dwordx4 %2, %0\n\t"
      "global_load_lds_dwordx4 %3, %0 offset:1024\n\t"
      "global_load_lds_dwordx4 %4, %0 offset:2048\n\t"
      "global_load_lds_dwordx4 %5, %0 offset:3072" ::"s"(sbase),
      "s"(m0v), "v"(v0), "v"(v1), "v"(v2), "v"(v3)
      : "memory");
}
DI void glds3(const char* sbase, u32 m0v, u32 v0, u32 v1, u32 v2) {
  asm volatile(
      "s_mov_b32 m0, %1\n\ts_nop 0\n\t"
      "global_load_lds_dwordx4 %2, %0\n\t"
      "global_load_lds_dwordx4 %3, %0 offset:1024\n\t"
      "global_load_lds_dwordx4 %4, %0 offset:2048" ::"s"(sbase),
      "s"(m0v), "v"(v0), "v"(v1), "v"(v2)
      : "memory");
}
DI void glds2(const char* sbase, u32 m0v, u32 v0, u32 v1) {
  asm volatile(
      "s_mov_b32 m0, %1\n\ts_nop 0\n\t"
      "global_load_lds_dwordx4 %2, %0\n\t"
      "global_load_lds_dwordx4 %3, %0 offset:1024" ::"s"(sbase),
      "s"(m0v), "v"(v0), "v"(v1)
      : "memory");
}
DI int kstep(int q, int c, int n) {
  if (q == 0) return c;
  int t = q - 1;
  if (t >= n - 4 + c) return q;
  return ((t & 3) == c) ? t + 4 : t;
}

constexpr int K0_LN = 256, K0_MOD = 96 * KS, K0_RS = 512;
__global__ __launch_bounds__(512) void k0_prep(const u16* __restrict__ x, const u16* __restrict__ wsre,
                                               const u16* __restrict__ wsim, const u16* __restrict__ cvec,
                                               const u16* __restrict__ wmod, char* __restrict__ ws) {
  __shared__ __attribute__((aligned(16))) u16 sm[8 * 1032];
  const int b = blockIdx.x, tid = threadIdx.x, lane = tid & 63, w = tid >> 6;
  if (b < K0_LN) {
    const int row = b * 8 + w;
    const u32x4* px = (const u32x4*)(x + (size_t)row * D_);
    u32x4 v0 = px[lane], v1 = px[64 + lane];
    float f[16];
#pragma unroll
    for (int i = 0; i < 4; i++) {
      f[2 * i] = bf_lo(v0[i]); f[2 * i + 1] = bf_hi(v0[i]);
      f[8 + 2 * i] = bf_lo(v1[i]); f[8 + 2 * i + 1] = bf_hi(v1[i]);
    }
    float s = 0.f;
#pragma unroll
    for (int i = 0; i < 16; i++) s += f[i];
    const float m = wave_sum(s) * (1.f / D_);
    float q = 0.f;
#pragma unroll
    for (int i = 0; i < 16; i++) { f[i] -= m; q += f[i] * f[i]; }
    const float r = 1.f / sqrtf(wave_sum(q) * (1.f / D_) + 1e-5f);
    u32x4 o0, o1;
#pragma unroll
    for (int i = 0; i < 4; i++) {
      o0[i] = pack2(f[2 * i] * r, f[2 * i + 1] * r);
      o1[i] = pack2(f[8 + 2 * i] * r, f[8 + 2 * i + 1] * r);
    }
    *(u32x4*)(sm + w * 1032 + lane * 8) = o0;
    *(u32x4*)(sm + w * 1032 + 512 + lane * 8) = o1;
    __syncthreads();
    char* nt = ws + WS_NT;
    const int s0 = b * 8;
#pragma unroll
    for (int h = 0; h < 2; h++) {
      const int rho = tid + 512 * h;
      const int d = (rho & ~31) | (((rho >> 2) & 3) << 3) | (((rho >> 4) & 1) << 2) | (rho & 3);
      u32x4 o;
#pragma unroll
      for (int j = 0; j < 4; j++) o[j] = (u32)sm[(2 * j) * 1032 + d] | ((u32)sm[(2 * j + 1) * 1032 + d] << 16);
      const size_t off = (size_t)((rho >> 7) * (S_ / 64) + (s0 >> 6)) * 16384 + ((rho >> 4) & 7) * 2048 +
                         ((s0 >> 3) & 7) * 256 + (rho & 15) * 16;
      *(u32x4*)(nt + off) = o;
    }
  } else if (b < K0_LN + K0_MOD) {
    const int j = b - K0_LN, cb = j % 96, ks = j / 96;
    constexpr int KR = 1024 / KS;
    const int r0 = ks * KR + (tid >> 3);
    u32x4 v[KR / 64];
    float sc[KR / 64];
#pragma unroll
    for (int p = 0; p < KR / 64; p++) v[p] = *(const u32x4*)(wmod + (size_t)(r0 + p * 64) * 6144 + cb * 64 + (tid & 7) * 8);
#pragma unroll
    for (int p = 0; p < KR / 64; p++) {
      const float cv = bf_lo(cvec[r0 + p * 64]);
      sc[p] = rbf(cv / (1.f + __expf(-cv)));
    }
    float a[8];
#pragma unroll
    for (int i = 0; i < 8; i++) a[i] = 0.f;
#pragma unroll
    for (int p = 0; p < KR / 64; p++)
#pragma unroll
      for (int i = 0; i < 4; i++) {
        a[2 * i] += sc[p] * bf_lo(v[p][i]);
        a[2 * i + 1] += sc[p] * bf_hi(v[p][i]);
      }
#pragma unroll
    for (int i = 0; i < 8; i++) {
      a[i] += __shfl_xor(a[i], 8, 64);
      a[i] += __shfl_xor(a[i], 16, 64);
      a[i] += __shfl_xor(a[i], 32, 64);
    }
    float* sf = (float*)sm;
    if (lane < 8) {
#pragma unroll
      for (int i = 0; i < 8; i++) sf[w * 64 + lane * 8 + i] = a[i];
    }
    __syncthreads();
    if (tid < 64) {
      float s = 0.f;
#pragma unroll
      for (int i = 0; i < 8; i++) s += sf[i * 64 + tid];
      ((float*)(ws + WS_MODP))[ks * 6144 + cb * 64 + tid] = s;
    }
  } else {
    const int row = (b - K0_LN - K0_MOD) * 8 + w;
    const u32x4* p = (const u32x4*)((row < S_ ? wsre : wsim) + (size_t)(row & (S_ - 1)) * S_);
    u32x4 v[4];
#pragma unroll
    for (int i = 0; i < 4; i++) v[i] = p[i * 64 + lane];
    float s = 0.f;
#pragma unroll
    for (int i = 0; i < 4; i++)
#pragma unroll
      for (int k = 0; k < 4; k++) s += bf_lo(v[i][k]) + bf_hi(v[i][k]);
    s = wave_sum(s);
    if (lane == 0) ((float*)(ws + WS_RS))[row] = s;
  }
}

template <int G>
struct Cfg;
template <> struct Cfg<1> { static constexpr int WT = 128, N = 32, NS = 4; };
template <> struct Cfg<2> { static constexpr int WT = 64, N = 32, NS = 5; };
template <> struct Cfg<4> { static constexpr int WT = 64, N = 56, NS = 5; };

template <int G>
__global__ __launch_bounds__(512) void gemm_pc(const char* __restrict__ W0, const char* __restrict__ W1,
                                               const char* __restrict__ xin, char* __restrict__ ws,
                                               char* __restrict__ out) {
  constexpr int WT = Cfg<G>::WT, N = Cfg<G>::N, NS = Cfg<G>::NS;
  constexpr int WB = WT * 128, STAGE = WB + 16384, WF = WT / 32, LPS = 4 + WF;
  constexpr int AUX = NS * STAGE;
  extern __shared__ __attribute__((aligned(16))) char smem[];
  const u32 lds0 = (u32)(size_t)(LDS_AS char*)smem;
  const int b = blockIdx.x, xcd = b & 7, r = b >> 3, ri = r & 3, rj = r >> 2;
  const int tid = threadIdx.x, lane = tid & 63;
  const int w = __builtin_amdgcn_readfirstlane(tid >> 6);
  int tx, tw;
  if (G == 1) { tw = xcd * 4 + ri; tx = rj; }
  else { tx = (xcd & 3) * 4 + ri; tw = (xcd >> 2) * 8 + rj; }
  const char* X = ws + (G == 1 ? WS_NT : G == 2 ? WS_PP : WS_P) + (size_t)tx * N * 16384;

  if (w >= 4) {
    const int lw = w - 4, c = (ri + rj) & 3;
    const u32 vx = lane * 16 + lw * 4096;
    u32 vw[4];
    const char* Wb;
    if (G == 1) {
      Wb = (tw < 16 ? W0 : W1) + (size_t)(tw & 15) * 128 * 4096;
      const int rr = lane >> 3;
#pragma unroll
      for (int i = 0; i < 4; i++) vw[i] = ((lw * 4 + i) * 8 + rr) * 4096 + (((lane & 7) ^ rr) << 4) - i * 1024;
    } else if (G == 2) {
      Wb = W0;
      const int rr = lane >> 3;
#pragma unroll
      for (int i = 0; i < 2; i++) {
        const int row = tw * 64 + (lw >> 1) * 32 + (lw & 1) * 4 + i * 16 + (rr >> 2) * 8 + (rr & 3);
        vw[i] = row * 2048 + (((lane & 7) ^ rr) << 4) - i * 1024;
      }
    } else {
      Wb = W0 + tw * 128;
#pragma unroll
      for (int i = 0; i < 2; i++) {
        const int row = (lw * 2 + i) * 8 + (lane >> 3);
        const int sw = (((row >> 1) & 1) | (((row >> 3) & 1) << 1)) << 1;
        vw[i] = row * 2048 + (((lane & 7) ^ sw) << 4) - i * 1024;
      }
    }
    auto issue = [&](int q) {
      const int s = kstep(q, c, N);
      const u32 sb = lds0 + (q % NS) * STAGE;
      const char* wp;
      if (G == 1) wp = Wb + s * 128;
      else if (G == 2) wp = (s < 16 ? W0 : W1) + (s & 15) * 128;
      else wp = Wb + (size_t)s * (64 * 2048);
      if (WF == 4) glds4(wp, sb + lw * 4096, vw[0], vw[1], vw[2], vw[3]);
      else glds2(wp, sb + lw * 2048, vw[0], vw[1]);
      glds4(X + (size_t)s * 16384, sb + WB + lw * 4096, vx, vx, vx, vx);
    };
#pragma unroll
    for (int q = 0; q < NS; q++) issue(q);
    WAIT_V((NS - 1) * LPS);
    BAR();
    for (int k = 0; k < N - NS; k++) {
      WAIT_V((NS - 2) * LPS);
      BAR();
      issue(k + NS);
    }
#pragma unroll
    for (int k = 0; k < NS - 1; k++) {
      WAIT_V((NS - 2 - k) * LPS);
      BAR();
    }
    return;
  }

  const int wx = w >> 1, ww = w & 1, g = lane >> 4, cc = lane & 15;
  float* aux = (float*)(smem + AUX);
  const float* modp = (const float*)(ws + WS_MODP);
  u32x4 res[4];
  if (G == 1) {
    const int d = tx * 128 + (tid & 127);
    aux[tid] = tid < 128 ? rbf(1.f + modval(modp, 1024 + d)) : modval(modp, d);
    if (tid < 128) aux[256 + tid] = ((const float*)(ws + WS_RS))[tw * 128 + tid];
  } else {
    if (tid < 64) aux[tid] = modval(modp, (G == 2 ? 2048 : 5120) + tw * 64 + tid);
#pragma unroll
    for (int m = 0; m < 4; m++) {
      if (G == 2) {
        res[m] = *(const u32x4*)(xin + (size_t)(tx * 128 + wx * 64 + m * 16 + cc) * 2048 + (tw * 64 + ww * 32 + g * 8) * 2);
      } else {
        const int gs = ww * 4 + (g & 1) * 2 + (g >> 1);
        res[m] = *(const u32x4*)(ws + WS_X2 + (size_t)(tx * 16 + tw) * 16384 + (wx * 4 + m) * 2048 + gs * 256 + cc * 16);
      }
    }
  }
  const u32 xa = lds0 + WB + wx * 8192 + lane * 16;
  u32 wa[2];
  if (G != 4) {
#pragma unroll
    for (int kk = 0; kk < 2; kk++) wa[kk] = lds0 + (ww * (WT / 2) + cc) * 128 + (((kk * 4 + g) ^ (cc & 7)) << 4);
  } else {
    const int q = cc >> 2, p = lane & 3;
    const int sw = ((q >> 1) | ((g & 1) << 1)) << 1;
#pragma unroll
    for (int n = 0; n < 2; n++) wa[n] = lds0 + (g * 8 + q) * 128 + (((ww * 4 + n * 2 + (p >> 1)) ^ sw) << 4) + (p & 1) * 8;
  }
  f32x4 acc[4][WF];
#pragma unroll
  for (int m = 0; m < 4; m++)
#pragma unroll
    for (int n = 0; n < WF; n++) acc[m][n] = f32x4{0.f, 0.f, 0.f, 0.f};
  bf16x8 fx[2][4], fw[2][WF];
  auto rd = [&](int kk, u32 so) {
#pragma unroll
    for (int n = 0; n < WF; n++) {
      if (G != 4) fw[kk][n] = lds_b128(wa[kk] + so + n * 2048);
      else fw[kk][n] = lds_tr2(wa[n] + so + kk * 4096, wa[n] + so + kk * 4096 + 512);
    }
#pragma unroll
    for (int m = 0; m < 4; m++) fx[kk][m] = lds_b128(xa + so + m * 2048 + kk * 1024);
  };
  auto mm = [&](int kk) {
#pragma unroll
    for (int m = 0; m < 4; m++)
#pragma unroll
      for (int n = 0; n < WF; n++)
        acc[m][n] = (G == 1) ? mfma16(fx[kk][m], fw[kk][n], acc[m][n]) : mfma16(fw[kk][n], fx[kk][m], acc[m][n]);
  };
  auto sched = [&]() {
    constexpr int NR = (G == 4 ? 2 * WF : WF) + 4, NM = 4 * WF;
#pragma unroll
    for (int i = 0; i < NR; i++) {
      __builtin_amdgcn_sched_group_barrier(0x008, 1, 0);
      __builtin_amdgcn_sched_group_barrier(0x100, 1, 0);
    }
    if (NM > NR) __builtin_amdgcn_sched_group_barrier(0x008, NM - NR, 0);
  };
  LBAR();
  rd(0, 0);
  u32 so = 0;
  for (int k = 0; k < N - 1; k++) {
    rd(1, so);
    mm(0);
    sched();
    __builtin_amdgcn_sched_barrier(0);
    LBAR();
    __builtin_amdgcn_sched_barrier(0);
    so += STAGE;
    if (so == NS * STAGE) so = 0;
    rd(0, so);
    mm(1);
    sched();
    __builtin_amdgcn_sched_barrier(0);
  }
  rd(1, so);
  mm(0);
  mm(1);

  if (G == 1) {
    const float sg = tw < 16 ? 1.f : -1.f;
    char* pp = ws + WS_PP + (size_t)((tw & 15) * 32 + (tw >> 4) * 16 + tx * 2 + wx) * 16384 + lane * 16;
#pragma unroll
    for (int h = 0; h < 2; h++) {
      const int d0 = wx * 64 + h * 32 + g * 8;
      const f32x4 c0 = *(const f32x4*)(aux + d0), c1 = *(const f32x4*)(aux + d0 + 4);
      const f32x4 s0 = *(const f32x4*)(aux + 128 + d0), s1 = *(const f32x4*)(aux + 128 + d0 + 4);
#pragma unroll
      for (int n = 0; n < WF; n++) {
        const float rsv = aux[256 + ww * 64 + n * 16 + cc];
        const f32x4 a = (acc[2 * h][n] * c0 + s0 * rsv) * sg, bb = (acc[2 * h + 1][n] * c1 + s1 * rsv) * sg;
        u32x4 o = {pack2(a[0], a[1]), pack2(a[2], a[3]), pack2(bb[0], bb[1]), pack2(bb[2], bb[3])};
        *(u32x4*)(pp + (ww * 4 + n) * 2048 + h * 1024) = o;
      }
    }
  } else if (G == 2) {
    const f32x4 g0 = *(const f32x4*)(aux + ww * 32 + g * 8), g1 = *(const f32x4*)(aux + ww * 32 + g * 8 + 4);
    char* px = ws + WS_X2 + (size_t)(tx * 16 + tw) * 16384 + ww * 1024 + lane * 16;
#pragma unroll
    for (int m = 0; m < 4; m++) {
      const f32x4 a = acc[m][0] * g0, bb = acc[m][1] * g1;
      u32x4 o = {pack2(bf_lo(res[m][0]) + a[0], bf_hi(res[m][0]) + a[1]), pack2(bf_lo(res[m][1]) + a[2], bf_hi(res[m][1]) + a[3]),
                 pack2(bf_lo(res[m][2]) + bb[0], bf_hi(res[m][2]) + bb[1]), pack2(bf_lo(res[m][3]) + bb[2], bf_hi(res[m][3]) + bb[3])};
      *(u32x4*)(px + (wx * 4 + m) * 2048) = o;
    }
  } else {
    const int n0 = ww * 32 + (g & 1) * 16 + (g >> 1) * 8;
    const f32x4 g0 = *(const f32x4*)(aux + n0), g1 = *(const f32x4*)(aux + n0 + 4);
#pragma unroll
    for (int m = 0; m < 4; m++) {
      f32x4 a, bb;
#pragma unroll
      for (int i = 0; i < 4; i++) {
        auto sw = __builtin_amdgcn_permlane16_swap(f2u(acc[m][0][i]), f2u(acc[m][1][i]), false, false);
        a[i] = u2f(sw[0]);
        bb[i] = u2f(sw[1]);
      }
      a *= g0; bb *= g1;
      u32x4 o = {pack2(bf_lo(res[m][0]) + a[0], bf_hi(res[m][0]) + a[1]), pack2(bf_lo(res[m][1]) + a[2], bf_hi(res[m][1]) + a[3]),
                 pack2(bf_lo(res[m][2]) + bb[0], bf_hi(res[m][2]) + bb[1]), pack2(bf_lo(res[m][3]) + bb[2], bf_hi(res[m][3]) + bb[3])};
      *(u32x4*)(out + (size_t)(tx * 128 + wx * 64 + m * 16 + cc) * 2048 + (tw * 64 + n0) * 2) = o;
    }
  }
}

__global__ __launch_bounds__(512) void k_ln2(char* __restrict__ ws) {
  __shared__ float red[2][8][16];
  __shared__ __attribute__((aligned(16))) float mv[2][1024];
  const int b = blockIdx.x, tid = threadIdx.x, lane = tid & 63, w = tid >> 6, g = lane >> 4, cc = lane & 15;
  const size_t base = (size_t)((b >> 3) * 16 + w * 2) * 16384 + (b & 7) * 2048 + lane * 16;
  u32x4 v[4];
#pragma unroll
  for (int i = 0; i < 4; i++) v[i] = *(const u32x4*)(ws + WS_X2 + base + (i >> 1) * 16384 + (i & 1) * 1024);
  const float* modp = (const float*)(ws + WS_MODP);
#pragma unroll
  for (int h = 0; h < 2; h++) {
    mv[0][tid + h * 512] = rbf(1.f + modval(modp, 4096 + tid + h * 512));
    mv[1][tid + h * 512] = modval(modp, 3072 + tid + h * 512);
  }
  float f[32];
  float s = 0.f;
#pragma unroll
  for (int i = 0; i < 4; i++)
#pragma unroll
    for (int k = 0; k < 4; k++) {
      f[i * 8 + 2 * k] = bf_lo(v[i][k]);
      f[i * 8 + 2 * k + 1] = bf_hi(v[i][k]);
      s += f[i * 8 + 2 * k] + f[i * 8 + 2 * k + 1];
    }
  s += __shfl_xor(s, 16, 64);
  s += __shfl_xor(s, 32, 64);
  if (lane < 16) red[0][w][cc] = s;
  __syncthreads();
  float m = 0.f;
#pragma unroll
  for (int i = 0; i < 8; i++) m += red[0][i][cc];
  m *= (1.f / D_);
  float q = 0.f;
#pragma unroll
  for (int i = 0; i < 32; i++) { f[i] -= m; q += f[i] * f[i]; }
  q += __shfl_xor(q, 16, 64);
  q += __shfl_xor(q, 32, 64);
  if (lane < 16) red[1][w][cc] = q;
  __syncthreads();
  float var = 0.f;
#pragma unroll
  for (int i = 0; i < 8; i++) var += red[1][i][cc];
  const float r = 1.f / sqrtf(var * (1.f / D_) + 1e-5f);
#pragma unroll
  for (int i = 0; i < 4; i++) {
    const int e = w * 128 + i * 32 + g * 8;
    u32x4 o;
#pragma unroll
    for (int k = 0; k < 4; k++)
      o[k] = pack2(f[i * 8 + 2 * k] * r * mv[0][e + 2 * k] + mv[1][e + 2 * k],
                   f[i * 8 + 2 * k + 1] * r * mv[0][e + 2 * k + 1] + mv[1][e + 2 * k + 1]);
    *(u32x4*)(ws + WS_H2 + base + (i >> 1) * 16384 + (i & 1) * 1024) = o;
  }
}

constexpr int G3_UP = 14336 + 16, G3_WS = 2 * G3_UP, G3_WOFF = 65536, G3_LDS = G3_WOFF + 3 * G3_WS;
DI float geglu(float gt, float up) {
  const float k1 = -2.302208198f, k3 = -0.1029432397f;
  const float e = __builtin_amdgcn_exp2f(gt * (k1 + k3 * gt * gt));
  return gt * __builtin_amdgcn_rcpf(1.f + e) * up;
}
__global__ __launch_bounds__(512) void gemm_g3(const char* __restrict__ Wg, const char* __restrict__ Wu,
                                               char* __restrict__ ws) {
  constexpr int N = 16;
  extern __shared__ __attribute__((aligned(16))) char smem[];
  const u32 lds0 = (u32)(size_t)(LDS_AS char*)smem;
  const int b = blockIdx.x, xcd = b & 7, r = b >> 3, ri = r & 3, rj = r >> 2;
  const int tm = (xcd & 1) * 4 + ri, tn = (xcd >> 1) * 8 + rj, c = (ri + rj) & 3;
  const int tid = threadIdx.x, lane = tid & 63;
  const int w = __builtin_amdgcn_readfirstlane(tid >> 6);
  const int grp = w >> 2, lw = w & 3, wx = lw >> 1, wn = lw & 1, g = lane >> 4, cc = lane & 15;

  u32 vo[7];
  const char* gb;
  if (grp == 0) {
    gb = (lw < 2 ? Wg : Wu) + tn * 224;
#pragma unroll
    for (int i = 0; i < 7; i++) {
      const int pi = ((lw & 1) * 7 + i) * 64 + lane, pr = pi / 14, ch = pi - pr * 14;
      const int k = (pr & ~12) | ((pr & 4) << 1) | ((pr & 8) >> 1);
      vo[i] = k * 7168 + ch * 16 - (i & 3) * 1024;
    }
  } else {
    gb = ws + WS_H2 + (size_t)tm * 2 * N * 16384;
#pragma unroll
    for (int i = 0; i < 7; i++) vo[i] = lane * 16 + lw * 4096;
  }
  auto issueW = [&](int q, int slot) {
    const char* p = gb + (size_t)kstep(q, c, N) * (64 * 7168);
    const u32 d = lds0 + G3_WOFF + slot * G3_WS + (lw >> 1) * G3_UP + (lw & 1) * 7168;
    glds4(p, d, vo[0], vo[1], vo[2], vo[3]);
    glds3(p, d + 4096, vo[4], vo[5], vo[6]);
  };
  auto issueX = [&](int q, int half) {
    const char* p = gb + (size_t)(half * N + kstep(q, c, N)) * 16384;
    glds4(p, lds0 + half * 32768 + (q & 1) * 16384 + lw * 4096, vo[0], vo[0], vo[0], vo[0]);
  };
  const u32 xa = lds0 + grp * 32768 + wx * 8192 + lane * 16;
  const int q4 = cc >> 2, p4 = lane & 3;
  const u32 rowb = lds0 + G3_WOFF + ((g >> 1) * 16 + (g & 1) * 4 + q4) * 224 + (p4 & 1) * 8;
  const u32 wa = rowb + (p4 >> 1) * 16 + wn * 96;
  const u32 wm = rowb + (p4 >> 1) * G3_UP + (12 + wn) * 16;
  f32x4 acc[4][7];
#pragma unroll
  for (int m = 0; m < 4; m++)
#pragma unroll
    for (int n = 0; n < 7; n++) acc[m][n] = f32x4{0.f, 0.f, 0.f, 0.f};
  bf16x8 fx[2][4], fw[2][7];
  auto rd = [&](int k, int slot) {
    const u32 xo = xa + (k & 1) * 16384, wo = wa + slot * G3_WS, mo = wm + slot * G3_WS;
#pragma unroll
    for (int kk = 0; kk < 2; kk++) {
#pragma unroll
      for (int m = 0; m < 4; m++) fx[kk][m] = lds_b128(xo + m * 2048 + kk * 1024);
#pragma unroll
      for (int n = 0; n < 6; n++) {
        const u32 a = wo + kk * 7168 + (n / 3) * G3_UP + (n % 3) * 32;
        fw[kk][n] = lds_tr2(a, a + 1792);
      }
      fw[kk][6] = lds_tr2(mo + kk * 7168, mo + kk * 7168 + 1792);
    }
  };
  auto mm = [&]() {
    __builtin_amdgcn_s_setprio(1);
#pragma unroll
    for (int kk = 0; kk < 2; kk++)
#pragma unroll
      for (int n = 0; n < 7; n++)
#pragma unroll
        for (int m = 0; m < 4; m++) acc[m][n] = mfma16(fw[kk][n], fx[kk][m], acc[m][n]);
    __builtin_amdgcn_s_setprio(0);
  };
#define SB() __builtin_amdgcn_sched_barrier(0)
  if (grp == 0) {
    issueW(0, 0);
    issueW(1, 1);
    WAIT_V(7);
    BAR();
    int slot = 0;
    for (int k = 0; k < N; k++) {
      const int s2 = slot == 0 ? 2 : slot - 1;
      if (k < N - 2) issueW(k + 2, s2);
      rd(k, slot);
      SB(); LBAR(); SB();
      mm();
      SB();
      if (k < N - 2) WAIT_V(7); else WAIT_V(0);
      BAR(); SB();
      slot = slot == 2 ? 0 : slot + 1;
    }
  } else {
    issueX(0, 0);
    issueX(0, 1);
    issueX(1, 0);
    WAIT_V(8);
    BAR();
    WAIT_V(4);
    BAR();
    int slot = 0;
    for (int k = 0; k < N; k++) {
      if (k < N - 1) issueX(k + 1, 1);
      if (k < N - 2) issueX(k + 2, 0);
      rd(k, slot);
      SB();
      if (k < N - 2) WAIT_V(8); else WAIT_V(4);
      LBAR(); SB();
      mm();
      SB();
      if (k < N - 1) {
        if (k < N - 2) WAIT_V(4); else WAIT_V(0);
        BAR();
      }
      SB();
      slot = slot == 2 ? 0 : slot + 1;
    }
  }
  char* pb = ws + WS_P + (size_t)(tm * 2 + grp) * 56 * 16384 + wx * 8192 + cc * 16;
  auto st = [&](int mt, int chunk, u32x4 v) {
    const int Gs = tn * 14 + chunk;
    *(u32x4*)(pb + (Gs >> 3) * 16384 + mt * 2048 + (Gs & 7) * 256) = v;
  };
  u32 t2[4][2];
#pragma unroll
  for (int m = 0; m < 4; m++) {
    u32 rr[3][2];
#pragma unroll
    for (int t = 0; t < 3; t++)
#pragma unroll
      for (int j = 0; j < 2; j++)
        rr[t][j] = pack2(geglu(acc[m][t][2 * j], acc[m][3 + t][2 * j]), geglu(acc[m][t][2 * j + 1], acc[m][3 + t][2 * j + 1]));
    auto s0 = __builtin_amdgcn_permlane16_swap(rr[0][0], rr[1][0], false, false);
    auto s1 = __builtin_amdgcn_permlane16_swap(rr[0][1], rr[1][1], false, false);
    st(m, wn * 6 + (g & 1) * 2 + (g >> 1), u32x4{s0[0], s1[0], s0[1], s1[1]});
    t2[m][0] = rr[2][0];
    t2[m][1] = rr[2][1];
  }
  u32 mx[2][2];
#pragma unroll
  for (int h = 0; h < 2; h++) {
    auto s0 = __builtin_amdgcn_permlane16_swap(t2[2 * h][0], t2[2 * h + 1][0], false, false);
    auto s1 = __builtin_amdgcn_permlane16_swap(t2[2 * h][1], t2[2 * h + 1][1], false, false);
    st(2 * h + (g & 1), wn * 6 + 4 + (g >> 1), u32x4{s0[0], s1[0], s0[1], s1[1]});
    float o[4];
#pragma unroll
    for (int i = 0; i < 4; i++) {
      auto sw = __builtin_amdgcn_permlane32_swap(f2u(acc[2 * h][6][i]), f2u(acc[2 * h + 1][6][i]), false, false);
      o[i] = geglu(u2f(sw[0]), u2f(sw[1]));
    }
    mx[h][0] = pack2(o[0], o[1]);
    mx[h][1] = pack2(o[2], o[3]);
  }
  {
    auto s0 = __builtin_amdgcn_permlane16_swap(mx[0][0], mx[1][0], false, false);
    auto s1 = __builtin_amdgcn_permlane16_swap(mx[0][1], mx[1][1], false, false);
    st((g & 1) * 2 + (g >> 1), 12 + wn, u32x4{s0[0], s1[0], s0[1], s1[1]});
  }
}

extern "C" void kernel_launch(void* const* d_in, const int* in_sizes, int n_in, void* d_out, int out_size, void* d_ws,
                              size_t ws_size, hipStream_t stream) {
  const char* x = (const char*)d_in[0];
  char* ws = (char*)d_ws;
  constexpr int L1 = Cfg<1>::NS * (Cfg<1>::WT * 128 + 16384) + 2048;
  constexpr int L2 = Cfg<2>::NS * (Cfg<2>::WT * 128 + 16384) + 2048;
  constexpr int L4 = Cfg<4>::NS * (Cfg<4>::WT * 128 + 16384) + 2048;
  (void)hipFuncSetAttribute((const void*)gemm_pc<1>, hipFuncAttributeMaxDynamicSharedMemorySize, L1);
  (void)hipFuncSetAttribute((const void*)gemm_pc<2>, hipFuncAttributeMaxDynamicSharedMemorySize, L2);
  (void)hipFuncSetAttribute((const void*)gemm_pc<4>, hipFuncAttributeMaxDynamicSharedMemorySize, L4);
  (void)hipFuncSetAttribute((const void*)gemm_g3, hipFuncAttributeMaxDynamicSharedMemorySize, G3_LDS);
  k0_prep<<<K0_LN + K0_MOD + K0_RS, 512, 0, stream>>>((const u16*)x, (const u16*)d_in[1], (const u16*)d_in[2],
                                                      (const u16*)d_in[9], (const u16*)d_in[10], ws);
  gemm_pc<1><<<256, 512, L1, stream>>>((const char*)d_in[1], (const char*)d_in[2], x, ws, (char*)d_out);
  gemm_pc<2><<<256, 512, L2, stream>>>((const char*)d_in[3], (const char*)d_in[4], x, ws, (char*)d_out);
  k_ln2<<<128, 512, 0, stream>>>(ws);
  gemm_g3<<<256, 512, G3_LDS, stream>>>((const char*)d_in[6], (const char*)d_in[7], ws);
  gemm_pc<4><<<256, 512, L4, stream>>>((const char*)d_in[8], (const char*)d_in[8], x, ws, (char*)d_out);
}
```

```cpp
#include <hip/hip_runtime.h>
#include <hip/hip_bf16.h>

typedef unsigned short u16;
typedef unsigned int u32;
typedef __bf16 bf16x8 __attribute__((ext_vector_type(8)));
typedef __bf16 bf16x2 __attribute__((ext_vector_type(2)));
typedef short s16x4 __attribute__((ext_vector_type(4)));
typedef float f32x4 __attribute__((ext_vector_type(4)));
typedef float f32x2 __attribute__((ext_vector_type(2)));
typedef u32 u32x4 __attribute__((ext_vector_type(4)));
typedef u32 u32x2 __attribute__((ext_vector_type(2)));
#define LDS_AS __attribute__((address_space(3)))
#define DI __device__ __forceinline__

constexpr int S_ = 2048, D_ = 1024, F_ = 3584;
constexpr int KS = 8;

constexpr size_t WS_MODP = 0;
constexpr size_t WS_ST = 262144;
constexpr size_t WS_NT = 1u << 20;
constexpr size_t WS_PP = WS_NT + (4u << 20);
constexpr size_t WS_X2 = WS_PP + (8u << 20);
constexpr size_t WS_H2 = WS_X2 + (4u << 20);
constexpr size_t WS_P = WS_H2 + (4u << 20);

DI float bf_lo(u32 v) { return __builtin_bit_cast(float, v << 16); }
DI float bf_hi(u32 v) { return __builtin_bit_cast(float, v & 0xffff0000u); }
DI u32 pack2(float a, float b) {
  f32x2 v = {a, b};
  return __builtin_bit_cast(u32, __builtin_convertvector(v, bf16x2));
}
DI u32 f2u(float a) { return __builtin_bit_cast(u32, a); }
DI float u2f(u32 a) { return __builtin_bit_cast(float, a); }
DI float rbf(float a) { return bf_lo(pack2(a, a)); }
DI float wave_sum(float v) {
#pragma unroll
  for (int o = 32; o; o >>= 1) v += __shfl_xor(v, o, 64);
  return v;
}
DI f32x4 mfma16(bf16x8 a, bf16x8 b, f32x4 c) { return __builtin_amdgcn_mfma_f32_16x16x32_bf16(a, b, c, 0, 0, 0); }
DI bf16x8 lds_b128(u32 addr) { return *(const LDS_AS bf16x8*)(size_t)addr; }
DI bf16x8 lds_tr2(u32 a0, u32 a1) {
  s16x4 lo = __builtin_amdgcn_ds_read_tr16_b64_v4i16((LDS_AS s16x4*)(size_t)a0);
  s16x4 hi = __builtin_amdgcn_ds_read_tr16_b64_v4i16((LDS_AS s16x4*)(size_t)a1);
  u32x2 l = __builtin_bit_cast(u32x2, lo), h = __builtin_bit_cast(u32x2, hi);
  u32x4 r = {l[0], l[1], h[0], h[1]};
  return __builtin_bit_cast(bf16x8, r);
}
#define WAIT_V(n) asm volatile("s_waitcnt vmcnt(%0)" ::"i"(n) : "memory")
#define BAR() asm volatile("s_barrier" ::: "memory")
#define LBAR() asm volatile("s_waitcnt lgkmcnt(0)\n\ts_barrier" ::: "memory")

#define STAMP_DECL
#define STAMP(i)
#define STAMP_OUT(kid, wv, n)

DI void glds4(const char* sbase, u32 m0v, u32 v0, u32 v1, u32 v2, u32 v3) {
  asm volatile(
      "s_mov_b32 m0, %1\n\ts_nop 0\n\t"
      "global_load_lds_dwordx4 %2, %0\n\t"
      "global_load_lds_dwordx4 %3, %0 offset:1024\n\t"
      "global_load_lds_dwordx4 %4, %0 offset:2048\n\t"
      "global_load_lds_dwordx4 %5, %0 offset:3072" ::"s"(sbase),
      "s"(m0v), "v"(v0), "v"(v1), "v"(v2), "v"(v3)
      : "memory");
}
DI void glds3(const char* sbase, u32 m0v, u32 v0, u32 v1, u32 v2) {
  asm volatile(
      "s_mov_b32 m0, %1\n\ts_nop 0\n\t"
      "global_load_lds_dwordx4 %2, %0\n\t"
      "global_load_lds_dwordx4 %3, %0 offset:1024\n\t"
      "global_load_lds_dwordx4 %4, %0 offset:2048" ::"s"(sbase),
      "s"(m0v), "v"(v0), "v"(v1), "v"(v2)
      : "memory");
}
DI void glds2(const char* sbase, u32 m0v, u32 v0, u32 v1) {
  asm volatile(
      "s_mov_b32 m0, %1\n\ts_nop 0\n\t"
      "global_load_lds_dwordx4 %2, %0\n\t"
      "global_load_lds_dwordx4 %3, %0 offset:1024" ::"s"(sbase),
      "s"(m0v), "v"(v0), "v"(v1)
      : "memory");
}
template <int OFF>
DI void glds1(const char* sbase, u32 m0v, u32 v0) {
  asm volatile("s_mov_b32 m0, %1\n\ts_nop 0\n\tglobal_load_lds_dwordx4 %2, %0 offset:%3" ::"s"(sbase), "s"(m0v), "v"(v0),
               "i"(OFF)
               : "memory");
}
DI void glds_dw(const char* sbase, u32 m0v, u32 v0) {
  asm volatile("s_mov_b32 m0, %1\n\ts_nop 0\n\tglobal_load_lds_dword %2, %0" ::"s"(sbase), "s"(m0v), "v"(v0) : "memory");
}
DI void glds_dw4(const char* sbase, u32 m0v, u32 v0, u32 v1, u32 v2, u32 v3) {
  asm volatile(
      "s_mov_b32 m0, %1\n\ts_nop 0\n\t"
      "global_load_lds_dword %2, %0\n\t"
      "global_load_lds_dword %3, %0 offset:256\n\t"
      "global_load_lds_dword %4, %0 offset:512\n\t"
      "global_load_lds_dword %5, %0 offset:768" ::"s"(sbase),
      "s"(m0v), "v"(v0), "v"(v1), "v"(v2), "v"(v3)
      : "memory");
}

typedef __amdgpu_buffer_rsrc_t rsrc_t;
DI rsrc_t mkrsrc(const void* p, int bytes = 0x4000000) { return __builtin_amdgcn_make_buffer_rsrc((void*)p, 0, bytes, 0x00020000); }
DI void st16(rsrc_t r, u32 off, u32x4 v) { __builtin_amdgcn_raw_buffer_store_b128(v, r, off, 0, 16); }
DI void stouch2(const char* p) {
  asm volatile("s_load_dwordx2 s[94:95], %0, 60\n\ts_load_dwordx2 s[94:95], %0, 188" ::"s"(p) : "s94", "s95");
}
DI int kstep0(int q, int c, int n) {
  if (q == 0) return c;
  int t = q - 1;
  if (t + 4 >= n + ((t - c) & 3) && t >= c) return q;
  return ((t & 3) == c) ? t + 4 : t;
}
template <int KM>
DI int kstep(int q, int c, int n) {
  if (KM == 0) return kstep0(q, c, n);
  if (KM == 9) return q;
  return q < KM ? q : KM + kstep0(q - KM, c, n - KM);
}
#define KMP 1
#define KM3 2

constexpr u32 EMPTY = 0xffffffffu;
__global__ __launch_bounds__(512) void k0_prep(const u16* __restrict__ x, const u16* __restrict__ cvec,
                                               const u16* __restrict__ wmod, char* __restrict__ ws) {
  __shared__ __attribute__((aligned(16))) u16 sm[8 * 1032];
  __shared__ float sf[8 * 192];
  STAMP_DECL
  const int b = blockIdx.x, tid = threadIdx.x, lane = tid & 63, w = tid >> 6;
  const rsrc_t rw = mkrsrc(ws);
  const u32x4* px = (const u32x4*)(x + (size_t)(b * 8 + w) * D_);
  const u32x4 v0 = px[lane], v1 = px[64 + lane];
  const int ks = b >> 5, cb = b & 31, r0 = ks * 128 + w * 16 + (lane >> 3);
  const u32 c0 = cvec[r0], c1 = cvec[r0 + 8];
  u32x4 wv[6];
#pragma unroll
  for (int i = 0; i < 6; i++)
    wv[i] = *(const u32x4*)(wmod + (size_t)(r0 + (i / 3) * 8) * 6144 + cb * 192 + (i % 3) * 64 + (lane & 7) * 8);
  __builtin_amdgcn_sched_barrier(0);
  {
    float f[16];
#pragma unroll
    for (int i = 0; i < 4; i++) {
      f[2 * i] = bf_lo(v0[i]); f[2 * i + 1] = bf_hi(v0[i]);
      f[8 + 2 * i] = bf_lo(v1[i]); f[8 + 2 * i + 1] = bf_hi(v1[i]);
    }
    float s = 0.f;
#pragma unroll
    for (int i = 0; i < 16; i++) s += f[i];
    const float m = wave_sum(s) * (1.f / D_);
    float q = 0.f;
#pragma unroll
    for (int i = 0; i < 16; i++) { f[i] -= m; q += f[i] * f[i]; }
    const float r = 1.f / sqrtf(wave_sum(q) * (1.f / D_) + 1e-5f);
    u32x4 o0, o1;
#pragma unroll
    for (int i = 0; i < 4; i++) {
      o0[i] = pack2(f[2 * i] * r, f[2 * i + 1] * r);
      o1[i] = pack2(f[8 + 2 * i] * r, f[8 + 2 * i + 1] * r);
    }
    *(u32x4*)(sm + w * 1032 + lane * 8) = o0;
    *(u32x4*)(sm + w * 1032 + 512 + lane * 8) = o1;
    LBAR();
    const int s0 = b * 8;
#pragma unroll
    for (int h = 0; h < 2; h++) {
      const int rho = tid + 512 * h;
      const int d = (rho & ~31) | (((rho >> 2) & 3) << 3) | (((rho >> 4) & 1) << 2) | (rho & 3);
      u32x4 o;
#pragma unroll
      for (int j = 0; j < 4; j++) o[j] = (u32)sm[(2 * j) * 1032 + d] | ((u32)sm[(2 * j + 1) * 1032 + d] << 16);
      const u32 off = ((rho >> 7) * (S_ / 64) + (s0 >> 6)) * 16384 + ((rho >> 4) & 7) * 2048 + ((s0 >> 3) & 7) * 256 + (rho & 15) * 16;
      st16(rw, WS_NT + off, o);
    }
    if (w == 0) st16(rw, WS_ST + b * 1024 + lane * 16, u32x4{EMPTY, EMPTY, EMPTY, EMPTY});
  }
  __builtin_amdgcn_sched_barrier(0);
  STAMP(1);
  {
    const float f0 = bf_lo(c0), f1 = bf_lo(c1);
    const float s0 = rbf(f0 / (1.f + __expf(-f0))), s1 = rbf(f1 / (1.f + __expf(-f1)));
    float a[24];
#pragma unroll
    for (int j = 0; j < 3; j++)
#pragma unroll
      for (int i = 0; i < 4; i++) {
        a[j * 8 + 2 * i] = s0 * bf_lo(wv[j][i]) + s1 * bf_lo(wv[3 + j][i]);
        a[j * 8 + 2 * i + 1] = s0 * bf_hi(wv[j][i]) + s1 * bf_hi(wv[3 + j][i]);
      }
#pragma unroll
    for (int i = 0; i < 12; i++) {
      auto t = __builtin_amdgcn_permlane32_swap(f2u(a[i]), f2u(a[i + 12]), false, false);
      a[i] = u2f(t[0]) + u2f(t[1]);
    }
#pragma unroll
    for (int i = 0; i < 6; i++) {
      auto t = __builtin_amdgcn_permlane16_swap(f2u(a[i]), f2u(a[i + 6]), false, false);
      a[i] = u2f(t[0]) + u2f(t[1]);
      a[i] += u2f(__builtin_amdgcn_update_dpp(0u, f2u(a[i]), 0x128, 0xf, 0xf, true));
    }
    if (!(lane & 8)) {
#pragma unroll
      for (int i = 0; i < 6; i++) {
        const int idx = i + 6 * (lane >> 4);
        sf[w * 192 + (idx >> 3) * 64 + (lane & 7) * 8 + (idx & 7)] = a[i];
      }
    }
    LBAR();
    if (tid < 192) {
      float s = 0.f;
#pragma unroll
      for (int i = 0; i < 8; i++) s += sf[i * 192 + tid];
      __builtin_amdgcn_raw_buffer_store_b32(f2u(s), rw, WS_MODP + (ks * 6144 + cb * 192 + tid) * 4, 0, 16);
    }
  }
  STAMP_OUT(0, w, 2);
}

template <int G>
struct Cfg;
#define A1 3
#define A2 3
#define A4 3
template <> struct Cfg<1> { static constexpr int WT = 128, N = 32, A = A1; };
template <> struct Cfg<2> { static constexpr int WT = 64, N = 32, A = A2; };
template <> struct Cfg<4> { static constexpr int WT = 64, N = 56, A = A4; };
constexpr int AUX_BYTES = 13312;

template <int G>
__global__ __launch_bounds__(512, 2) void gemm_pc(const char* __restrict__ W0, const char* __restrict__ W1,
                                               const char* __restrict__ xin, char* __restrict__ ws,
                                               char* __restrict__ out, const char* __restrict__ pf0,
                                               const char* __restrict__ pf1) {
  constexpr int WT = Cfg<G>::WT, N = Cfg<G>::N, A = Cfg<G>::A, NS = A + 1;
  constexpr int WB = WT * 128, STAGE = WB + 16384, WF = WT / 32, LPS = 4 + WF;
  constexpr int AUX = NS * STAGE, RES = (N % NS) * STAGE + WB;
  STAMP_DECL
  extern __shared__ __attribute__((aligned(16))) char smem[];
  const u32 lds0 = (u32)(size_t)(LDS_AS char*)smem;
  const int b = blockIdx.x, xcd = b & 7, r = b >> 3, ri = r & 3, rj = r >> 2;
  const int tid = threadIdx.x, lane = tid & 63;
  const int w = __builtin_amdgcn_readfirstlane(tid >> 6);
  int tx, tw;
  if (G == 1) { tw = xcd * 4 + ri; tx = rj; }
  else { tx = (xcd & 3) * 4 + ri; tw = (xcd >> 2) * 8 + rj; }
  const char* X = ws + (G == 1 ? WS_NT : G == 2 ? WS_PP : WS_P) + (size_t)tx * N * 16384;
  float* aux = (float*)(smem + AUX);

  if (w >= 4) {
    const int lw = w - 4, c = (ri + rj) & 3;
    const u32 vx = lane * 16 + lw * 4096;
    u32 vw[4];
    const char* Wb;
    const int rr = lane >> 3;
    if (G == 1) {
      Wb = (tw < 16 ? W0 : W1) + (size_t)(tw & 15) * 128 * 4096;
#pragma unroll
      for (int i = 0; i < 4; i++) vw[i] = ((lw * 4 + i) * 8 + rr) * 4096 + (((lane & 7) ^ rr) << 4) - i * 1024;
    } else if (G == 2) {
      Wb = W0;
#pragma unroll
      for (int i = 0; i < 2; i++) {
        const int row = tw * 64 + (lw >> 1) * 32 + (lw & 1) * 4 + i * 16 + (rr >> 2) * 8 + (rr & 3);
        vw[i] = row * 2048 + (((lane & 7) ^ rr) << 4) - i * 1024;
      }
    } else {
      Wb = W0 + tw * 128;
#pragma unroll
      for (int i = 0; i < 2; i++) {
        const int row = (lw * 2 + i) * 8 + rr;
        const int sw = (((row >> 1) & 1) | (((row >> 3) & 1) << 1)) << 1;
        vw[i] = row * 2048 + (((lane & 7) ^ sw) << 4) - i * 1024;
      }
    }
    auto issue = [&](int q) {
      const int s = kstep<KMP>(q, c, N);
      const u32 sb = lds0 + (q % NS) * STAGE;
      const char* wp;
      if (G == 1) wp = Wb + s * 128;
      else if (G == 2) wp = (s < 16 ? W0 : W1) + (s & 15) * 128;
      else wp = Wb + (size_t)s * (64 * 2048);
      if (WF == 4) glds4(wp, sb + lw * 4096, vw[0], vw[1], vw[2], vw[3]);
      else glds2(wp, sb + lw * 2048, vw[0], vw[1]);
      glds4(X + (size_t)s * 16384, sb + WB + lw * 4096, vx, vx, vx, vx);
    };
    STAMP(1);
    issue(0);
    issue(1);
    STAMP(2);
    WAIT_V(LPS);
    BAR();
    STAMP(3);
#pragma unroll
    for (int q = 2; q < A; q++) issue(q);
    const int gw = b * 4 + lw;
    constexpr int TJ = G == 1 ? 16 : 15;
    const char* t0 = G == 1 ? (gw < 512 ? pf0 : pf1) + (gw & 511) * 4096 : pf0 + gw * 7168 + 26 * 128;
    const char* t1 = (G == 1 ? xin : pf1) + gw * 7168 - TJ * 256;
    for (int j = 0; j < N - A; j++) {
      issue(j + A);
      if (G != 4 && j < 29) stouch2((j < TJ ? t0 : t1) + j * 256);
      WAIT_V((A - 1) * LPS);
      BAR();
    }
    constexpr int NT = G == 1 ? 8 : 12;
    {
      const int col = G == 1 ? (lw >> 1) * 1024 + tx * 128 + (lw & 1) * 64 : (G == 2 ? 2048 + lw * 1024 : 5120) + tw * 64;
      const u32 v = (col + lane) * 4;
#pragma unroll
      for (int h = 0; h < 2; h++)
        glds_dw4(ws + WS_MODP + h * 4 * 24576, lds0 + AUX + 2048 + lw * 2048 + h * 1024, v, v + 24576 - 256,
                 v + 2 * (24576 - 256), v + 3 * (24576 - 256));
      if (G == 2) {
        const u32 u = (lw * 32 + rr) * 2048 + (((lane & 7) ^ rr) << 4);
        glds4(xin + (size_t)tx * 128 * 2048 + tw * 128, lds0 + RES + lw * 4096, u, u + 16384 - 1024, u + 2 * (16384 - 1024),
              u + 3 * (16384 - 1024));
      } else if (G == 4) {
        glds4(ws + WS_X2 + (size_t)(tx * 16 + tw) * 16384, lds0 + RES + lw * 4096, vx, vx, vx, vx);
      }
    }
#pragma unroll
    for (int k = 0; k < A - 1; k++) {
      WAIT_V((A - 2 - k) * LPS + NT);
      BAR();
    }
    WAIT_V(0);
    {
      const float* raw = aux + 512 + lw * 512 + lane;
      const float v = rbf(((raw[0] + raw[64]) + (raw[128] + raw[192])) + ((raw[256] + raw[320]) + (raw[384] + raw[448])));
      aux[lw * 64 + lane] = lw < 2 ? v : rbf(1.f + v);
    }
    LBAR();
    STAMP(4);
    if (G == 2) {
      const rsrc_t rw = mkrsrc(ws);
      const int row = lw * 32 + (lane & 31);
      BAR();
      {
        const f32x2 p = *(const f32x2*)(aux + 2560 + row * 2), q = *(const f32x2*)(aux + 2816 + row * 2);
        const float d = p[0] - q[0];
        u32 m = f2u(0.5f * (p[0] + q[0])), v = f2u(p[1] + q[1] + d * d * 16.f);
        m = m == EMPTY ? 0x7fc00000u : m;
        v = v == EMPTY ? 0x7fc00000u : v;
        if (lane < 32) __builtin_amdgcn_raw_buffer_store_b64(u32x2{m, v}, rw, WS_ST + ((tx * 16 + tw) * 128 + row) * 8, 0, 16);
      }
      const u32 go = WS_ST + ((tx * 16 + (lane >> 5) * 8) * 128 + row) * 8;
      u32 gm[8], gq[8];
#pragma unroll
      for (int i = 0; i < 8; i++) gm[i] = gq[i] = EMPTY;
      const unsigned long long tb = __builtin_amdgcn_s_memrealtime();
      while (true) {
        asm volatile("" ::: "memory");
        u32x2 nv[8];
#pragma unroll
        for (int i = 0; i < 8; i++)
          nv[i] = __builtin_amdgcn_raw_buffer_load_b64(rw, max(gm[i], gq[i]) == EMPTY ? go + i * 1024 : 0x7ffffff0u, 0, 16);
        __builtin_amdgcn_sched_barrier(0);
        u32 mx = 0;
#pragma unroll
        for (int i = 0; i < 8; i++) {
          const bool miss = max(gm[i], gq[i]) == EMPTY;
          const u32 a0 = nv[i][0], a1 = nv[i][1];
          gm[i] = miss ? a0 : gm[i];
          gq[i] = miss ? a1 : gq[i];
          mx = max(mx, max(gm[i], gq[i]));
        }
        if (!__builtin_amdgcn_ballot_w64(mx == EMPTY) || __builtin_amdgcn_s_memrealtime() - tb > 2000000ull) break;
        __builtin_amdgcn_s_sleep(2);
      }
      float sm = 0.f, q = 0.f;
#pragma unroll
      for (int i = 0; i < 8; i++) sm += u2f(gm[i]);
      sm = (sm + __shfl_xor(sm, 32, 64)) * (1.f / 16);
#pragma unroll
      for (int i = 0; i < 8; i++) {
        const float d = u2f(gm[i]) - sm;
        q += u2f(gq[i]) + 64.f * d * d;
      }
      q += __shfl_xor(q, 32, 64);
      if (lane < 32) *(f32x2*)(aux + 3072 + row * 2) = f32x2{sm, 1.f / sqrtf(q * (1.f / D_) + 1e-5f)};
      LBAR();
      if (lane < 28) glds_dw(pf1, lds0 + AUX + 2048 + lw * 1024, gw * 7168 + (28 + lane) * 128);
    }
    WAIT_V(0);
    STAMP_OUT(G, w, 5);
    return;
  }

  const int wx = w >> 1, ww = w & 1, g = lane >> 4, cc = lane & 15;
  const u32 xa = lds0 + WB + wx * 8192 + lane * 16;
  u32 wa[2], wb[2];
  if (G != 4) {
#pragma unroll
    for (int kk = 0; kk < 2; kk++) {
      const u32 a = lds0 + (ww * (WT / 2) + cc) * 128 + (((kk * 4 + g) ^ (cc & 7)) << 4);
      wa[kk] = a + (G == 1 ? wx * 4096 : 0);
      wb[kk] = a + (1 - wx) * 4096;
    }
  } else {
    const int q = cc >> 2, p = lane & 3;
    const int sw = ((q >> 1) | ((g & 1) << 1)) << 1;
#pragma unroll
    for (int n = 0; n < 2; n++) wa[n] = lds0 + (g * 8 + q) * 128 + (((ww * 4 + n * 2 + (p >> 1)) ^ sw) << 4) + (p & 1) * 8;
  }
  f32x4 acc[4][WF];
#pragma unroll
  for (int m = 0; m < 4; m++)
#pragma unroll
    for (int n = 0; n < WF; n++) acc[m][n] = f32x4{0.f, 0.f, 0.f, 0.f};
  bf16x8 fx[2][4], fw[2][WF];
  auto rd = [&](int kk, u32 so) {
#pragma unroll
    for (int n = 0; n < WF; n++) {
      if (G == 1) fw[kk][n] = lds_b128((n < 2 ? wa[kk] : wb[kk]) + so + (n & 1) * 2048);
      else if (G == 2) fw[kk][n] = lds_b128(wa[kk] + so + n * 2048);
      else fw[kk][n] = lds_tr2(wa[n] + so + kk * 4096, wa[n] + so + kk * 4096 + 512);
    }
#pragma unroll
    for (int m = 0; m < 4; m++) fx[kk][m] = lds_b128(xa + so + m * 2048 + kk * 1024);
  };
  f32x4 rsum[2] = {f32x4{0.f, 0.f, 0.f, 0.f}, f32x4{0.f, 0.f, 0.f, 0.f}};
  const bf16x8 ones = __builtin_bit_cast(bf16x8, u32x4{0x3f803f80u, 0x3f803f80u, 0x3f803f80u, 0x3f803f80u});
  auto mm = [&](int kk) {
#pragma unroll
    for (int m = 0; m < 4; m++)
#pragma unroll
      for (int i = 0; i < WF; i++) {
        const int n = (m & 1) ? WF - 1 - i : i;
        acc[m][n] = (G == 1) ? mfma16(fx[kk][m], fw[kk][n], acc[m][n]) : mfma16(fw[kk][n], fx[kk][m], acc[m][n]);
      }
    if (G == 1) {
      rsum[0] = mfma16(ones, fw[kk][0], rsum[0]);
      rsum[1] = mfma16(ones, fw[kk][1], rsum[1]);
    }
  };
  auto sched = [&]() {
    constexpr int NR = (G == 4 ? 2 * WF : WF) + 4, NM = 4 * WF + (G == 1 ? 2 : 0);
#pragma unroll
    for (int i = 0; i < NR; i++) {
      __builtin_amdgcn_sched_group_barrier(0x008, 1, 0);
      __builtin_amdgcn_sched_group_barrier(0x100, 1, 0);
    }
    if (NM > NR) __builtin_amdgcn_sched_group_barrier(0x008, NM - NR, 0);
  };
  STAMP(1);
  BAR();
  STAMP(2);
  u32 so = 0;
  rd(0, so);
  for (int k = 0; k < N - 1; k++) {
    rd(1, so);
    mm(0);
    sched();
    __builtin_amdgcn_sched_barrier(0);
    LBAR();
    __builtin_amdgcn_sched_barrier(0);
    so += STAGE;
    if (so == NS * STAGE) so = 0;
    rd(0, so);
    mm(1);
    sched();
    __builtin_amdgcn_sched_barrier(0);
  }
  rd(1, so);
  mm(0);
  mm(1);
  if (G == 1 && lane < 32) aux[256 + ww * 64 + wx * 32 + lane] = lane < 16 ? rsum[0][0] : rsum[1][0];
  __builtin_amdgcn_sched_barrier(0);
  LBAR();
  STAMP(3);
  u32x4 res[4];
  if (G != 1) {
#pragma unroll
    for (int m = 0; m < 4; m++) {
      const u32 o = G == 2 ? (wx * 64 + m * 16 + cc) * 128 + (((ww * 4 + g) ^ (cc & 7)) << 4)
                           : (wx * 4 + m) * 2048 + (ww * 4 + (g & 1) * 2 + (g >> 1)) * 256 + cc * 16;
      res[m] = *(const u32x4*)(smem + RES + o);
    }
  }

  const rsrc_t rw = mkrsrc(ws);
  if (G == 1) {
    const float sg = tw < 16 ? 1.f : -1.f;
    const u32 pp = WS_PP + ((tw & 15) * 32 + (tw >> 4) * 16 + tx * 2 + wx) * 16384 + lane * 16;
#pragma unroll
    for (int h = 0; h < 2; h++) {
      const int d0 = wx * 64 + h * 32 + g * 8;
      const f32x4 s0 = *(const f32x4*)(aux + d0) * sg, s1 = *(const f32x4*)(aux + d0 + 4) * sg;
      const f32x4 c0 = *(const f32x4*)(aux + 128 + d0) * sg, c1 = *(const f32x4*)(aux + 128 + d0 + 4) * sg;
#pragma unroll
      for (int n = 0; n < WF; n++) {
        const int t = ww * 4 + (n ^ (wx * 2));
        const float rsv = aux[256 + t * 16 + cc];
        const f32x4 a = acc[2 * h][n] * c0 + s0 * rsv, bb = acc[2 * h + 1][n] * c1 + s1 * rsv;
        u32x4 o = {pack2(a[0], a[1]), pack2(a[2], a[3]), pack2(bb[0], bb[1]), pack2(bb[2], bb[3])};
        st16(rw, pp + t * 2048 + h * 1024, o);
      }
    }
  } else if (G == 2) {
    const int e0 = ww * 32 + g * 8;
    const f32x4 g0 = *(const f32x4*)(aux + e0), g1 = *(const f32x4*)(aux + e0 + 4);
    const u32 px = (tx * 16 + tw) * 16384 + wx * 8192 + ww * 1024 + lane * 16;
    u32x4 o[4];
    float f[4][8], mean[4], m2[4];
#pragma unroll
    for (int m = 0; m < 4; m++) {
      const f32x4 a = acc[m][0] * g0, bb = acc[m][1] * g1;
      o[m] = u32x4{pack2(bf_lo(res[m][0]) + a[0], bf_hi(res[m][0]) + a[1]), pack2(bf_lo(res[m][1]) + a[2], bf_hi(res[m][1]) + a[3]),
                   pack2(bf_lo(res[m][2]) + bb[0], bf_hi(res[m][2]) + bb[1]), pack2(bf_lo(res[m][3]) + bb[2], bf_hi(res[m][3]) + bb[3])};
#pragma unroll
      for (int i = 0; i < 4; i++) { f[m][2 * i] = bf_lo(o[m][i]); f[m][2 * i + 1] = bf_hi(o[m][i]); }
      mean[m] = (((f[m][0] + f[m][1]) + (f[m][2] + f[m][3])) + ((f[m][4] + f[m][5]) + (f[m][6] + f[m][7]))) * 0.125f;
      m2[m] = 0.f;
#pragma unroll
      for (int i = 0; i < 8; i++) m2[m] += (f[m][i] - mean[m]) * (f[m][i] - mean[m]);
    }
#pragma unroll
    for (int m = 0; m < 2; m++) {
      auto t = __builtin_amdgcn_permlane32_swap(f2u(mean[m]), f2u(mean[m + 2]), false, false);
      auto u = __builtin_amdgcn_permlane32_swap(f2u(m2[m]), f2u(m2[m + 2]), false, false);
      const float d = u2f(t[0]) - u2f(t[1]);
      mean[m] = 0.5f * (u2f(t[0]) + u2f(t[1]));
      m2[m] = u2f(u[0]) + u2f(u[1]) + d * d * 4.f;
    }
    {
      auto t = __builtin_amdgcn_permlane16_swap(f2u(mean[0]), f2u(mean[1]), false, false);
      auto u = __builtin_amdgcn_permlane16_swap(f2u(m2[0]), f2u(m2[1]), false, false);
      const float d = u2f(t[0]) - u2f(t[1]);
      *(f32x2*)(aux + 2560 + ww * 256 + (wx * 64 + lane) * 2) =
          f32x2{0.5f * (u2f(t[0]) + u2f(t[1])), u2f(u[0]) + u2f(u[1]) + d * d * 8.f};
    }
    LBAR();
#pragma unroll
    for (int m = 0; m < 4; m++) st16(rw, WS_X2 + px + m * 2048, o[m]);
    const f32x4 s0 = *(const f32x4*)(aux + 64 + e0), s1 = *(const f32x4*)(aux + 64 + e0 + 4);
    const f32x4 c0 = *(const f32x4*)(aux + 128 + e0), c1 = *(const f32x4*)(aux + 128 + e0 + 4);
    STAMP(4);
    BAR();
#pragma unroll
    for (int m = 0; m < 4; m++) {
      const f32x2 st = *(const f32x2*)(aux + 3072 + (wx * 64 + m * 16 + cc) * 2);
      float h[8];
#pragma unroll
      for (int i = 0; i < 8; i++) h[i] = (f[m][i] - st[0]) * st[1] * (i < 4 ? c0[i & 3] : c1[i & 3]) + (i < 4 ? s0[i & 3] : s1[i & 3]);
      st16(rw, WS_H2 + px + m * 2048, u32x4{pack2(h[0], h[1]), pack2(h[2], h[3]), pack2(h[4], h[5]), pack2(h[6], h[7])});
    }
  } else {
    const rsrc_t ro = mkrsrc(out, S_ * D_ * 2);
    const int n0 = ww * 32 + (g & 1) * 16 + (g >> 1) * 8;
    const f32x4 g0 = *(const f32x4*)(aux + n0), g1 = *(const f32x4*)(aux + n0 + 4);
#pragma unroll
    for (int m = 0; m < 4; m++) {
      f32x4 a, bb;
#pragma unroll
      for (int i = 0; i < 4; i++) {
        auto sw = __builtin_amdgcn_permlane16_swap(f2u(acc[m][0][i]), f2u(acc[m][1][i]), false, false);
        a[i] = u2f(sw[0]);
        bb[i] = u2f(sw[1]);
      }
      a *= g0; bb *= g1;
      u32x4 o = {pack2(bf_lo(res[m][0]) + a[0], bf_hi(res[m][0]) + a[1]), pack2(bf_lo(res[m][1]) + a[2], bf_hi(res[m][1]) + a[3]),
                 pack2(bf_lo(res[m][2]) + bb[0], bf_hi(res[m][2]) + bb[1]), pack2(bf_lo(res[m][3]) + bb[2], bf_hi(res[m][3]) + bb[3])};
      st16(ro, (tx * 128 + wx * 64 + m * 16 + cc) * 2048 + (tw * 64 + n0) * 2, o);
    }
  }
  STAMP_OUT(G, w, (G == 2 ? 5 : 4));
}

constexpr int G3_UP = 14336 + 16, G3_WS = 2 * G3_UP, G3_WOFF = 65536, G3_PF = G3_WOFF + 3 * G3_WS, G3_LDS = G3_PF + 256;
template <int V> struct IC { static constexpr int value = V; };
DI float geglu(float gt, float up) {
  const float k1 = -2.302208198f, k3 = -0.1029432397f;
  const float e = __builtin_amdgcn_exp2f(gt * (k1 + k3 * gt * gt));
  return gt * __builtin_amdgcn_rcpf(1.f + e) * up;
}
__global__ __launch_bounds__(512) void gemm_g3(const char* __restrict__ Wg, const char* __restrict__ Wu,
                                               const char* __restrict__ Wd, char* __restrict__ ws) {
  constexpr int N = 16;
  STAMP_DECL
  extern __shared__ __attribute__((aligned(16))) char smem[];
  const u32 lds0 = (u32)(size_t)(LDS_AS char*)smem;
  const int b = blockIdx.x, xcd = b & 7, r = b >> 3, ri = r & 3, rj = r >> 2;
  const int tm = (xcd & 1) * 4 + ri, tn = (xcd >> 1) * 8 + rj, c = (ri + rj) & 3;
  const int tid = threadIdx.x, lane = tid & 63;
  const int w = __builtin_amdgcn_readfirstlane(tid >> 6);
  const int grp = w >> 2, lw = w & 3, wx = lw >> 1, wn = lw & 1, g = lane >> 4, cc = lane & 15;

  u32 vo[7];
  const char* gb;
  if (grp == 0) {
    gb = (lw < 2 ? Wg : Wu) + tn * 224;
#pragma unroll
    for (int i = 0; i < 7; i++) {
      const int pi = ((lw & 1) * 7 + i) * 64 + lane, pr = pi / 14, ch = pi - pr * 14;
      const int k = (pr & ~12) | ((pr & 4) << 1) | ((pr & 8) >> 1);
      vo[i] = k * 7168 + ch * 16 - (i & 3) * 1024;
    }
  } else {
    gb = ws + WS_H2 + (size_t)tm * 2 * N * 16384;
#pragma unroll
    for (int i = 0; i < 7; i++) vo[i] = lane * 16 + lw * 4096;
  }
  auto issueW = [&](int q, int slot) {
    const char* p = gb + (size_t)kstep<KM3>(q, c, N) * (64 * 7168);
    const u32 d = lds0 + G3_WOFF + slot * G3_WS + (lw >> 1) * G3_UP + (lw & 1) * 7168;
    glds4(p, d, vo[0], vo[1], vo[2], vo[3]);
    glds3(p, d + 4096, vo[4], vo[5], vo[6]);
  };
  auto issueX = [&](int q, int half) {
    const char* p = gb + (size_t)(half * N + kstep<KM3>(q, c, N)) * 16384;
    glds4(p, lds0 + half * 32768 + (q & 1) * 16384 + lw * 4096, vo[0], vo[0], vo[0], vo[0]);
  };
  const u32 xa = lds0 + grp * 32768 + wx * 8192 + lane * 16;
  const int q4 = cc >> 2, p4 = lane & 3;
  const u32 rowb = lds0 + G3_WOFF + ((g >> 1) * 16 + (g & 1) * 4 + q4) * 224 + (p4 & 1) * 8;
  const u32 wa = rowb + (p4 >> 1) * 16 + wn * 96;
  const u32 wm = rowb + (p4 >> 1) * G3_UP + (12 + wn) * 16;
  f32x4 acc[4][7];
#pragma unroll
  for (int m = 0; m < 4; m++)
#pragma unroll
    for (int n = 0; n < 7; n++) acc[m][n] = f32x4{0.f, 0.f, 0.f, 0.f};
  bf16x8 fx[2][4], fw[2][7];
  auto rd = [&](int k, int slot, auto np, const char* p0, u32 d0, const char* p1, u32 d1) {
    constexpr int NP = decltype(np)::value;
    const u32 xo = xa + (k & 1) * 16384, wo = wa + slot * G3_WS, mo = wm + slot * G3_WS;
    auto piece = [&](auto ic) {
      constexpr int I = decltype(ic)::value;
      if constexpr (I < NP) glds1<(I & 3) * 1024>(I < 4 ? p0 : p1, I < 4 ? d0 : d1, vo[I < 7 ? I : 0]);
    };
    auto rw = [&](int kk, int n) {
      const u32 a = wo + kk * 7168 + (n / 3) * G3_UP + (n % 3) * 32;
      fw[kk][n] = lds_tr2(a, a + 1792);
    };
    auto half = [&](auto kc) {
      constexpr int kk = decltype(kc)::value;
      piece(IC<kk * 4>{});
      fx[kk][0] = lds_b128(xo + kk * 1024); rw(kk, 0); rw(kk, 1);
      piece(IC<kk * 4 + 1>{});
      fx[kk][1] = lds_b128(xo + 2048 + kk * 1024); rw(kk, 2); rw(kk, 3);
      piece(IC<kk * 4 + 2>{});
      fx[kk][2] = lds_b128(xo + 4096 + kk * 1024); rw(kk, 4); rw(kk, 5);
      piece(IC<kk * 4 + 3>{});
      fx[kk][3] = lds_b128(xo + 6144 + kk * 1024);
      fw[kk][6] = lds_tr2(mo + kk * 7168, mo + kk * 7168 + 1792);
    };
    half(IC<0>{});
    half(IC<1>{});
  };
#define SB() __builtin_amdgcn_sched_barrier(0)
#define EB 1
  auto mm = [&](auto wv) {
    constexpr int WV = decltype(wv)::value;
    __builtin_amdgcn_s_setprio(1);
#pragma unroll
    for (int i = 0; i < 56; i++) {
      const int kk = i / 28, n = (i % 28) / 4, m = (n & 1) ? 3 - (i & 3) : (i & 3);
      if (i == 56 - EB && WV >= 0) {
        SB();
        WAIT_V(WV);
        BAR();
        if (EB > 1) __builtin_amdgcn_s_setprio(3);
        SB();
      }
      acc[m][n] = mfma16(fw[kk][n], fx[kk][m], acc[m][n]);
    }
    __builtin_amdgcn_s_setprio(0);
    SB();
  };
  if (grp == 0) {
    auto wsrc = [&](int q) { return gb + (size_t)kstep<KM3>(q, c, N) * (64 * 7168); };
    const u32 wd = lds0 + G3_WOFF + (lw >> 1) * G3_UP + (lw & 1) * 7168;
    issueW(0, 0);
    STAMP(1);
    WAIT_V(0);
    BAR();
    STAMP(2);
    issueW(1, 1);
    int slot = 0;
    for (int k = 0; k < N - 2; k++) {
      const int s2 = slot == 0 ? 2 : slot - 1;
      const char* p = wsrc(k + 2);
      rd(k, slot, IC<7>{}, p, wd + s2 * G3_WS, p, wd + s2 * G3_WS + 4096);
      if (k == 2 + 3 * lw && lane < 56) glds_dw(Wd, lds0 + G3_PF, ((b * 4 + lw) * 56 + lane) * 128);
      SB(); LBAR(); SB();
      mm(IC<7>{});
      slot = slot == 2 ? 0 : slot + 1;
    }
    rd(N - 2, (N - 2) % 3, IC<0>{}, gb, 0, gb, 0);
    SB(); LBAR(); SB();
    mm(IC<0>{});
    rd(N - 1, (N - 1) % 3, IC<0>{}, gb, 0, gb, 0);
    SB(); LBAR(); SB();
    mm(IC<0>{});
  } else {
    auto xsrc = [&](int q, int half) { return gb + (size_t)(half * N + kstep<KM3>(q, c, N)) * 16384; };
    const u32 xd = lds0 + lw * 4096;
    issueX(0, 0);
    issueX(0, 1);
    STAMP(1);
    WAIT_V(4);
    BAR();
    issueX(1, 0);
    WAIT_V(4);
    BAR();
    STAMP(2);
    int slot = 0;
    for (int k = 0; k < N - 2; k++) {
      rd(k, slot, IC<8>{}, xsrc(k + 1, 1), xd + 32768 + ((k + 1) & 1) * 16384, xsrc(k + 2, 0), xd + (k & 1) * 16384);
      SB();
      WAIT_V(8);
      LBAR(); SB();
      mm(IC<4>{});
      slot = slot == 2 ? 0 : slot + 1;
    }
    rd(N - 2, (N - 2) % 3, IC<4>{}, xsrc(N - 1, 1), xd + 32768 + ((N - 1) & 1) * 16384, gb, 0);
    SB();
    WAIT_V(4);
    LBAR(); SB();
    mm(IC<0>{});
    rd(N - 1, (N - 1) % 3, IC<0>{}, gb, 0, gb, 0);
    SB(); LBAR(); SB();
    mm(IC<-1>{});
  }
  STAMP(3);
  const rsrc_t rw = mkrsrc(ws);
  const u32 pb = WS_P + (tm * 2 + grp) * 56 * 16384 + wx * 8192 + cc * 16;
  auto st = [&](int mt, int chunk, u32x4 v) {
    const int Gs = tn * 14 + chunk;
    st16(rw, pb + (Gs >> 3) * 16384 + mt * 2048 + (Gs & 7) * 256, v);
  };
  u32 t2[4][2];
#pragma unroll
  for (int m = 0; m < 4; m++) {
    u32 rr[3][2];
#pragma unroll
    for (int t = 0; t < 3; t++)
#pragma unroll
      for (int j = 0; j < 2; j++)
        rr[t][j] = pack2(geglu(acc[m][t][2 * j], acc[m][3 + t][2 * j]), geglu(acc[m][t][2 * j + 1], acc[m][3 + t][2 * j + 1]));
    auto s0 = __builtin_amdgcn_permlane16_swap(rr[0][0], rr[1][0], false, false);
    auto s1 = __builtin_amdgcn_permlane16_swap(rr[0][1], rr[1][1], false, false);
    st(m, wn * 6 + (g & 1) * 2 + (g >> 1), u32x4{s0[0], s1[0], s0[1], s1[1]});
    t2[m][0] = rr[2][0];
    t2[m][1] = rr[2][1];
  }
  u32 mx[2][2];
#pragma unroll
  for (int h = 0; h < 2; h++) {
    auto s0 = __builtin_amdgcn_permlane16_swap(t2[2 * h][0], t2[2 * h + 1][0], false, false);
    auto s1 = __builtin_amdgcn_permlane16_swap(t2[2 * h][1], t2[2 * h + 1][1], false, false);
    st(2 * h + (g & 1), wn * 6 + 4 + (g >> 1), u32x4{s0[0], s1[0], s0[1], s1[1]});
    float o[4];
#pragma unroll
    for (int i = 0; i < 4; i++) {
      auto sw = __builtin_amdgcn_permlane32_swap(f2u(acc[2 * h][6][i]), f2u(acc[2 * h + 1][6][i]), false, false);
      o[i] = geglu(u2f(sw[0]), u2f(sw[1]));
    }
    mx[h][0] = pack2(o[0], o[1]);
    mx[h][1] = pack2(o[2], o[3]);
  }
  {
    auto s0 = __builtin_amdgcn_permlane16_swap(mx[0][0], mx[1][0], false, false);
    auto s1 = __builtin_amdgcn_permlane16_swap(mx[0][1], mx[1][1], false, false);
    st((g & 1) * 2 + (g >> 1), 12 + wn, u32x4{s0[0], s1[0], s0[1], s1[1]});
  }
  STAMP_OUT(3, w, 4);
}

extern "C" void kernel_launch(void* const* d_in, const int* in_sizes, int n_in, void* d_out, int out_size, void* d_ws,
                              size_t ws_size, hipStream_t stream) {
  const char* x = (const char*)d_in[0];
  char* ws = (char*)d_ws;
  constexpr int L1 = (Cfg<1>::A + 1) * (Cfg<1>::WT * 128 + 16384) + AUX_BYTES;
  constexpr int L2 = (Cfg<2>::A + 1) * (Cfg<2>::WT * 128 + 16384) + AUX_BYTES;
  constexpr int L4 = (Cfg<4>::A + 1) * (Cfg<4>::WT * 128 + 16384) + AUX_BYTES;
  (void)hipFuncSetAttribute((const void*)gemm_pc<1>, hipFuncAttributeMaxDynamicSharedMemorySize, L1);
  (void)hipFuncSetAttribute((const void*)gemm_pc<2>, hipFuncAttributeMaxDynamicSharedMemorySize, L2);
  (void)hipFuncSetAttribute((const void*)gemm_pc<4>, hipFuncAttributeMaxDynamicSharedMemorySize, L4);
  (void)hipFuncSetAttribute((const void*)gemm_g3, hipFuncAttributeMaxDynamicSharedMemorySize, G3_LDS);
  k0_prep<<<256, 512, 0, stream>>>((const u16*)x, (const u16*)d_in[9], (const u16*)d_in[10], ws);
  gemm_pc<1><<<256, 512, L1, stream>>>((const char*)d_in[1], (const char*)d_in[2], (const char*)d_in[6], ws,
                                       (char*)d_out, (const char*)d_in[3], (const char*)d_in[4]);
  gemm_pc<2><<<256, 512, L2, stream>>>((const char*)d_in[3], (const char*)d_in[4], x, ws, (char*)d_out,
                                       (const char*)d_in[6], (const char*)d_in[7]);
  gemm_g3<<<256, 512, G3_LDS, stream>>>((const char*)d_in[6], (const char*)d_in[7], (const char*)d_in[8], ws);
  gemm_pc<4><<<256, 512, L4, stream>>>((const char*)d_in[8], (const char*)d_in[8], x, ws, (char*)d_out, x, x);
}
```

```cpp
#include <hip/hip_runtime.h>
#include <hip/hip_bf16.h>

typedef unsigned short u16;
typedef unsigned int u32;
typedef __bf16 bf16x8 __attribute__((ext_vector_type(8)));
typedef __bf16 bf16x2 __attribute__((ext_vector_type(2)));
typedef short s16x4 __attribute__((ext_vector_type(4)));
typedef float f32x4 __attribute__((ext_vector_type(4)));
typedef float f32x2 __attribute__((ext_vector_type(2)));
typedef u32 u32x4 __attribute__((ext_vector_type(4)));
typedef u32 u32x2 __attribute__((ext_vector_type(2)));
#define LDS_AS __attribute__((address_space(3)))
#define DI __device__ __forceinline__

constexpr int S_ = 2048, D_ = 1024, F_ = 3584;
constexpr int KS = 8;

constexpr size_t WS_MODP = 0;
constexpr size_t WS_ST = 262144;
constexpr size_t WS_FL = 524288;
constexpr size_t WS_MODF = 540672;
constexpr size_t WS_NT = 1u << 20;
constexpr size_t WS_PP = WS_NT + (4u << 20);
constexpr size_t WS_X2 = WS_PP + (8u << 20);
constexpr size_t WS_H2 = WS_X2 + (4u << 20);
constexpr size_t WS_P = WS_H2 + (4u << 20);
constexpr size_t WS_XB = WS_P + (14u << 20);

DI float bf_lo(u32 v) { return __builtin_bit_cast(float, v << 16); }
DI float bf_hi(u32 v) { return __builtin_bit_cast(float, v & 0xffff0000u); }
DI u32 pack2(float a, float b) {
  f32x2 v = {a, b};
  return __builtin_bit_cast(u32, __builtin_convertvector(v, bf16x2));
}
DI u32 f2u(float a) { return __builtin_bit_cast(u32, a); }
DI float u2f(u32 a) { return __builtin_bit_cast(float, a); }
DI float rbf(float a) { return bf_lo(pack2(a, a)); }
DI float wave_sum(float v) {
#pragma unroll
  for (int o = 32; o; o >>= 1) v += __shfl_xor(v, o, 64);
  return v;
}
DI float sum32(float v) {
  auto t = __builtin_amdgcn_permlane32_swap(f2u(v), f2u(v), false, false);
  return u2f(t[0]) + u2f(t[1]);
}
DI f32x4 mfma16(bf16x8 a, bf16x8 b, f32x4 c) { return __builtin_amdgcn_mfma_f32_16x16x32_bf16(a, b, c, 0, 0, 0); }
DI bf16x8 lds_b128(u32 addr) { return *(const LDS_AS bf16x8*)(size_t)addr; }
DI bf16x8 lds_tr2(u32 a0, u32 a1) {
  s16x4 lo = __builtin_amdgcn_ds_read_tr16_b64_v4i16((LDS_AS s16x4*)(size_t)a0);
  s16x4 hi = __builtin_amdgcn_ds_read_tr16_b64_v4i16((LDS_AS s16x4*)(size_t)a1);
  u32x2 l = __builtin_bit_cast(u32x2, lo), h = __builtin_bit_cast(u32x2, hi);
  u32x4 r = {l[0], l[1], h[0], h[1]};
  return __builtin_bit_cast(bf16x8, r);
}
#define WAIT_V(n) asm volatile("s_waitcnt vmcnt(%0)" ::"i"(n) : "memory")
#define BAR() asm volatile("s_barrier" ::: "memory")
#define WAIT_L() __builtin_amdgcn_s_waitcnt(0xC07F)
#define LBAR() do { asm volatile("" ::: "memory"); WAIT_L(); BAR(); } while (0)

#define STAMP_DECL
#define STAMP(i)
#define STAMP_OUT(kid, wv, n)

#define TL_DECL
#define TL()
#define TL_OUT(kid, wv)

DI void glds4(const char* sbase, u32 m0v, u32 v0, u32 v1, u32 v2, u32 v3) {
  asm volatile(
      "s_mov_b32 m0, %1\n\ts_nop 0\n\t"
      "global_load_lds_dwordx4 %2, %0\n\t"
      "global_load_lds_dwordx4 %3, %0 offset:1024\n\t"
      "global_load_lds_dwordx4 %4, %0 offset:2048\n\t"
      "global_load_lds_dwordx4 %5, %0 offset:3072" ::"s"(sbase),
      "s"(m0v), "v"(v0), "v"(v1), "v"(v2), "v"(v3)
      : "memory");
}
DI void glds3(const char* sbase, u32 m0v, u32 v0, u32 v1, u32 v2) {
  asm volatile(
      "s_mov_b32 m0, %1\n\ts_nop 0\n\t"
      "global_load_lds_dwordx4 %2, %0\n\t"
      "global_load_lds_dwordx4 %3, %0 offset:1024\n\t"
      "global_load_lds_dwordx4 %4, %0 offset:2048" ::"s"(sbase),
      "s"(m0v), "v"(v0), "v"(v1), "v"(v2)
      : "memory");
}
DI void glds2(const char* sbase, u32 m0v, u32 v0, u32 v1) {
  asm volatile(
      "s_mov_b32 m0, %1\n\ts_nop 0\n\t"
      "global_load_lds_dwordx4 %2, %0\n\t"
      "global_load_lds_dwordx4 %3, %0 offset:1024" ::"s"(sbase),
      "s"(m0v), "v"(v0), "v"(v1)
      : "memory");
}
template <int OFF>
DI void glds1(const char* sbase, u32 m0v, u32 v0) {
  asm volatile("s_mov_b32 m0, %1\n\ts_nop 0\n\tglobal_load_lds_dwordx4 %2, %0 offset:%3" ::"s"(sbase), "s"(m0v), "v"(v0),
               "i"(OFF)
               : "memory");
}
DI void glds4_dev(const char* sbase, u32 m0v, u32 v0) {
  asm volatile(
      "s_mov_b32 m0, %1\n\ts_nop 0\n\t"
      "global_load_lds_dwordx4 %2, %0 sc1\n\t"
      "global_load_lds_dwordx4 %2, %0 offset:1024 sc1\n\t"
      "global_load_lds_dwordx4 %2, %0 offset:2048 sc1\n\t"
      "global_load_lds_dwordx4 %2, %0 offset:3072 sc1" ::"s"(sbase),
      "s"(m0v), "v"(v0)
      : "memory");
}
DI void glds_dw(const char* sbase, u32 m0v, u32 v0) {
  asm volatile("s_mov_b32 m0, %1\n\ts_nop 0\n\tglobal_load_lds_dword %2, %0" ::"s"(sbase), "s"(m0v), "v"(v0) : "memory");
}
DI void glds_dw4(const char* sbase, u32 m0v, u32 v0, u32 v1, u32 v2, u32 v3) {
  asm volatile(
      "s_mov_b32 m0, %1\n\ts_nop 0\n\t"
      "global_load_lds_dword %2, %0\n\t"
      "global_load_lds_dword %3, %0 offset:256\n\t"
      "global_load_lds_dword %4, %0 offset:512\n\t"
      "global_load_lds_dword %5, %0 offset:768" ::"s"(sbase),
      "s"(m0v), "v"(v0), "v"(v1), "v"(v2), "v"(v3)
      : "memory");
}

typedef __amdgpu_buffer_rsrc_t rsrc_t;
DI rsrc_t mkrsrc(const void* p, int bytes = 0x4000000) { return __builtin_amdgcn_make_buffer_rsrc((void*)p, 0, bytes, 0x00020000); }
DI void st16(rsrc_t r, u32 off, u32x4 v) { __builtin_amdgcn_raw_buffer_store_b128(v, r, off, 0, 16); }
DI void stouch2(const char* p) {
  asm volatile("s_load_dwordx2 s[94:95], %0, 60\n\ts_load_dwordx2 s[94:95], %0, 188" ::"s"(p) : "s94", "s95");
}
DI int kstep0(int q, int c, int n) {
  if (q == 0) return c;
  int t = q - 1;
  if (t + 4 >= n + ((t - c) & 3) && t >= c) return q;
  return ((t & 3) == c) ? t + 4 : t;
}
template <int KM>
DI int kstep(int q, int c, int n) {
  if (KM == 0) return kstep0(q, c, n);
  if (KM == 9) return q;
  return q < KM ? q : KM + kstep0(q - KM, c, n - KM);
}
#define KMP 1
#define KMS 9
#define KMO 0
#define KM3 2

constexpr u32 EMPTY = 0xffffffffu;
constexpr int SPIN_CAP = 1 << 16;
__global__ __launch_bounds__(512) void k0_prep(const u16* __restrict__ x, const u16* __restrict__ cvec,
                                               const u16* __restrict__ wmod, char* __restrict__ ws) {
  __shared__ __attribute__((aligned(16))) u16 sm[8 * 1032];
  __shared__ float sf[8 * 192];
  STAMP_DECL
  const int b = blockIdx.x, tid = threadIdx.x, lane = tid & 63, w = tid >> 6;
  const rsrc_t rw = mkrsrc(ws);
  const u32x4* px = (const u32x4*)(x + (size_t)(b * 8 + w) * D_);
  const u32x4 v0 = px[lane], v1 = px[64 + lane];
  const int ks = b >> 5, cb = b & 31, r0 = ks * 128 + w * 16 + (lane >> 3);
  const u32 c0 = cvec[r0], c1 = cvec[r0 + 8];
  u32x4 wv[6];
#pragma unroll
  for (int i = 0; i < 6; i++)
    wv[i] = *(const u32x4*)(wmod + (size_t)(r0 + (i / 3) * 8) * 6144 + cb * 192 + (i % 3) * 64 + (lane & 7) * 8);
  __builtin_amdgcn_sched_barrier(0);
  {
    float f[16];
#pragma unroll
    for (int i = 0; i < 4; i++) {
      f[2 * i] = bf_lo(v0[i]); f[2 * i + 1] = bf_hi(v0[i]);
      f[8 + 2 * i] = bf_lo(v1[i]); f[8 + 2 * i + 1] = bf_hi(v1[i]);
    }
    float s = 0.f;
#pragma unroll
    for (int i = 0; i < 16; i++) s += f[i];
    const float m = wave_sum(s) * (1.f / D_);
    float q = 0.f;
#pragma unroll
    for (int i = 0; i < 16; i++) { f[i] -= m; q += f[i] * f[i]; }
    const float r = 1.f / sqrtf(wave_sum(q) * (1.f / D_) + 1e-5f);
    u32x4 o0, o1;
#pragma unroll
    for (int i = 0; i < 4; i++) {
      o0[i] = pack2(f[2 * i] * r, f[2 * i + 1] * r);
      o1[i] = pack2(f[8 + 2 * i] * r, f[8 + 2 * i + 1] * r);
    }
    *(u32x4*)(sm + w * 1032 + lane * 8) = o0;
    *(u32x4*)(sm + w * 1032 + 512 + lane * 8) = o1;
    LBAR();
    const int s0 = b * 8;
#pragma unroll
    for (int h = 0; h < 2; h++) {
      const int rho = tid + 512 * h;
      const int d = (rho & ~31) | (((rho >> 2) & 3) << 3) | (((rho >> 4) & 1) << 2) | (rho & 3);
      u32x4 o;
#pragma unroll
      for (int j = 0; j < 4; j++) o[j] = (u32)sm[(2 * j) * 1032 + d] | ((u32)sm[(2 * j + 1) * 1032 + d] << 16);
      const u32 off = ((rho >> 7) * (S_ / 64) + (s0 >> 6)) * 16384 + ((rho >> 4) & 7) * 2048 + ((s0 >> 3) & 7) * 256 + (rho & 15) * 16;
      st16(rw, WS_NT + off, o);
    }
  }
  __builtin_amdgcn_sched_barrier(0);
  STAMP(1);
  {
    const float f0 = bf_lo(c0), f1 = bf_lo(c1);
    const float s0 = rbf(f0 / (1.f + __expf(-f0))), s1 = rbf(f1 / (1.f + __expf(-f1)));
    float a[24];
#pragma unroll
    for (int j = 0; j < 3; j++)
#pragma unroll
      for (int i = 0; i < 4; i++) {
        a[j * 8 + 2 * i] = s0 * bf_lo(wv[j][i]) + s1 * bf_lo(wv[3 + j][i]);
        a[j * 8 + 2 * i + 1] = s0 * bf_hi(wv[j][i]) + s1 * bf_hi(wv[3 + j][i]);
      }
#pragma unroll
    for (int i = 0; i < 12; i++) {
      auto t = __builtin_amdgcn_permlane32_swap(f2u(a[i]), f2u(a[i + 12]), false, false);
      a[i] = u2f(t[0]) + u2f(t[1]);
    }
#pragma unroll
    for (int i = 0; i < 6; i++) {
      auto t = __builtin_amdgcn_permlane16_swap(f2u(a[i]), f2u(a[i + 6]), false, false);
      a[i] = u2f(t[0]) + u2f(t[1]);
      a[i] += u2f(__builtin_amdgcn_update_dpp(0u, f2u(a[i]), 0x128, 0xf, 0xf, true));
    }
    if (!(lane & 8)) {
#pragma unroll
      for (int i = 0; i < 6; i++) {
        const int idx = i + 6 * (lane >> 4);
        sf[w * 192 + (idx >> 3) * 64 + (lane & 7) * 8 + (idx & 7)] = a[i];
      }
    }
    if (w == 7) st16(rw, WS_ST + b * 1024 + lane * 16, u32x4{EMPTY, EMPTY, EMPTY, EMPTY});
    if (tid == 384) st16(rw, WS_FL + b * 16, u32x4{EMPTY, EMPTY, EMPTY, EMPTY});
    LBAR();
    if (tid < 192) {
      float s = 0.f;
#pragma unroll
      for (int i = 0; i < 8; i++) s += sf[i * 192 + tid];
      __builtin_amdgcn_raw_buffer_store_b32(f2u(s), rw, WS_MODP + (ks * 6144 + cb * 192 + tid) * 4, 0, 16);
    }
  }
  STAMP_OUT(0, w, 2);
}

constexpr int STAGE = 32768, WB = 16384, AUX = 4 * STAGE;
constexpr int PC_LDS = AUX + 13312;
template <int V> struct IC { static constexpr int value = V; };
#define SB() __builtin_amdgcn_sched_barrier(0)
template <int N2, int NM>
DI void sched_rm() {
#pragma unroll
  for (int i = 0; i < NM; i++) {
    __builtin_amdgcn_sched_group_barrier(0x008, 1, 0);
    if (i < N2) __builtin_amdgcn_sched_group_barrier(0x100, 2, 0);
    else if (i < 8) __builtin_amdgcn_sched_group_barrier(0x100, 1, 0);
    else __builtin_amdgcn_sched_group_barrier(0x006, 2, 0);
  }
  SB();
}
DI void pin(u32& v) { asm volatile("" : "+v"(v)); }
DI void pin(f32x4& v) { asm volatile("" : "+v"(v)); }
DI void mod_load(const char* ws, u32 lds0, int lw, int col) {
  const u32 v = col * 4;
#pragma unroll
  for (int h = 0; h < 2; h++)
    glds_dw4(ws + WS_MODP + h * 4 * 24576, lds0 + AUX + 2048 + lw * 2048 + h * 1024, v, v + 24576 - 256,
             v + 2 * (24576 - 256), v + 3 * (24576 - 256));
}
DI void mod_final(float* aux, int lw, int lane) {
  const float* raw = aux + 512 + lw * 512 + lane;
  const float v = rbf(((raw[0] + raw[64]) + (raw[128] + raw[192])) + ((raw[256] + raw[320]) + (raw[384] + raw[448])));
  aux[lw * 64 + lane] = lw < 2 ? v : rbf(1.f + v);
}

__global__ __launch_bounds__(512, 2) void gemm_g1(const char* __restrict__ W0, const char* __restrict__ W1,
                                                  char* __restrict__ ws, const char* __restrict__ pf0,
                                                  const char* __restrict__ pf1, const char* __restrict__ pf2,
                                                  const char* __restrict__ pf3) {
  constexpr int N = 32;
  STAMP_DECL
  extern __shared__ __attribute__((aligned(16))) char smem[];
  const u32 lds0 = (u32)(size_t)(LDS_AS char*)smem;
  const int b = blockIdx.x, xcd = b & 7, r = b >> 3, ri = r & 3, rj = r >> 2;
  const int tid = threadIdx.x, lane = tid & 63;
  const int w = __builtin_amdgcn_readfirstlane(tid >> 6);
  const int tw = xcd * 4 + ri, tx = rj;
  float* aux = (float*)(smem + AUX);

  if (w >= 4) {
    const int lw = w - 4, c = (ri + rj) & 3, rr = lane >> 3;
    const u32 vx = lane * 16 + lw * 4096;
    const char* X = ws + WS_NT + (size_t)tx * N * 16384;
    const char* Wb = (tw < 16 ? W0 : W1) + (size_t)(tw & 15) * 128 * 4096;
    u32 vw[4];
#pragma unroll
    for (int i = 0; i < 4; i++) vw[i] = ((lw * 4 + i) * 8 + rr) * 4096 + (((lane & 7) ^ rr) << 4) - i * 1024;
    auto issue = [&](int q) {
      const int s = kstep<KMP>(q, c, N);
      const u32 sb = lds0 + (q & 3) * STAGE + lw * 4096;
      glds4(Wb + s * 128, sb, vw[0], vw[1], vw[2], vw[3]);
      glds4(X + (size_t)s * 16384, sb + WB, vx, vx, vx, vx);
    };
    STAMP(1);
    issue(0);
    issue(1);
    STAMP(2);
    WAIT_V(8);
    BAR();
    STAMP(3);
    issue(2);
    const int gw = b * 4 + lw;
    const char* t0 = (gw & 1 ? pf1 : pf0) + (gw >> 1) * 256;
    const char* t1 = (gw & 1 ? pf3 : pf2) + (gw >> 1) * 256 - 16 * 131072;
    for (int j = 0; j < N - 3; j++) {
      issue(j + 3);
      stouch2((j < 16 ? t0 : t1) + j * 131072);
      WAIT_V(16);
      BAR();
    }
    mod_load(ws, lds0, lw, (lw >> 1) * 1024 + tx * 128 + (lw & 1) * 64 + lane);
    WAIT_V(16);
    BAR();
    WAIT_V(8);
    BAR();
    WAIT_V(0);
    const int col = gw * 4 + (lane & 3);
    float v = *(const float*)(ws + WS_MODP + ((lane >> 3) * 6144 + 2048 + col) * 4);
    mod_final(aux, lw, lane);
    BAR();
    LBAR();
    v += u2f(__builtin_amdgcn_update_dpp(0u, f2u(v), 0x128, 0xf, 0xf, true));
    auto t = __builtin_amdgcn_permlane16_swap(f2u(v), f2u(v), false, false);
    v = rbf(sum32(u2f(t[0]) + u2f(t[1])));
    if ((col >> 10) == 2) v = rbf(1.f + v);
    if (lane < 4) __builtin_amdgcn_raw_buffer_store_b32(f2u(v), mkrsrc(ws), WS_MODF + col * 4, 0, 16);
    STAMP_OUT(1, w, 4);
    return;
  }

  TL_DECL
  const int wx = w >> 1, ww = w & 1, g = lane >> 4, cc = lane & 15;
  u32 xa = lds0 + WB + wx * 8192 + lane * 16, wa[4];
#pragma unroll
  for (int i = 0; i < 4; i++)
    wa[i] = lds0 + ww * 8192 + ((i & 1) ^ wx) * 4096 + cc * 128 + ((((i >> 1) * 4 + g) ^ (cc & 7)) << 4);
  f32x4 acc[4][4], rsum[2];
#pragma unroll
  for (int i = 0; i < 18; i++) {
    f32x4& v = i < 16 ? acc[i >> 2][i & 3] : rsum[i & 1];
    v = f32x4{0.f, 0.f, 0.f, 0.f};
    pin(v);
  }
  bf16x8 fx[2][4], fw[2][4];
  auto rd = [&](int kk, u32 so) {
#pragma unroll
    for (int n = 0; n < 4; n++) fw[kk][n] = lds_b128(wa[kk * 2 + (n >> 1)] + so + (n & 1) * 2048);
#pragma unroll
    for (int m = 0; m < 4; m++) fx[kk][m] = lds_b128(xa + so + m * 2048 + kk * 1024);
  };
  const bf16x8 ones = __builtin_bit_cast(bf16x8, u32x4{0x3f803f80u, 0x3f803f80u, 0x3f803f80u, 0x3f803f80u});
  auto mm = [&](int kk) {
#pragma unroll
    for (int m = 0; m < 4; m++)
#pragma unroll
      for (int i = 0; i < 4; i++) {
        const int n = (m & 1) ? 3 - i : i;
        acc[m][n] = mfma16(fx[kk][m], fw[kk][n], acc[m][n]);
      }
    rsum[0] = mfma16(ones, fw[kk][0], rsum[0]);
    rsum[1] = mfma16(ones, fw[kk][1], rsum[1]);
    sched_rm<0, 18>();
  };
  pin(xa); pin(wa[0]); pin(wa[1]); pin(wa[2]); pin(wa[3]);
  STAMP(1);
  LBAR(); TL();
  STAMP(2);
  rd(0, 0);
  SB();
  for (int k = 0; k < N; k += 2) {
    WAIT_L(); SB();
    rd(1, 0);
    mm(0);
    LBAR(); TL(); SB();
    rd(0, STAGE);
    mm(1);
    WAIT_L(); SB();
    rd(1, STAGE);
    xa ^= 2 * STAGE; wa[0] ^= 2 * STAGE; wa[1] ^= 2 * STAGE; wa[2] ^= 2 * STAGE; wa[3] ^= 2 * STAGE;
    mm(0);
    LBAR(); TL(); SB();
    rd(0, 0);
    mm(1);
  }
  if (lane < 32) aux[256 + ww * 64 + wx * 32 + lane] = lane < 16 ? rsum[0][0] : rsum[1][0];
  SB(); LBAR(); TL();
  STAMP(3);
  const rsrc_t rw = mkrsrc(ws);
  const float sg = tw < 16 ? 1.f : -1.f;
  const u32 pp = WS_PP + ((tw & 15) * 32 + (tw >> 4) * 16 + tx * 2 + wx) * 16384 + lane * 16;
#pragma unroll
  for (int h = 0; h < 2; h++) {
    const int d0 = wx * 64 + h * 32 + g * 8;
    const f32x4 s0 = *(const f32x4*)(aux + d0) * sg, s1 = *(const f32x4*)(aux + d0 + 4) * sg;
    const f32x4 c0 = *(const f32x4*)(aux + 128 + d0) * sg, c1 = *(const f32x4*)(aux + 128 + d0 + 4) * sg;
#pragma unroll
    for (int n = 0; n < 4; n++) {
      const int t = ww * 4 + (n ^ (wx * 2));
      const float rsv = aux[256 + t * 16 + cc];
      const f32x4 a = acc[2 * h][n] * c0 + s0 * rsv, bb = acc[2 * h + 1][n] * c1 + s1 * rsv;
      u32x4 o = {pack2(a[0], a[1]), pack2(a[2], a[3]), pack2(bb[0], bb[1]), pack2(bb[2], bb[3])};
      st16(rw, pp + t * 2048 + h * 1024, o);
    }
  }
  STAMP_OUT(1, w, 4);
  TL_OUT(1, w);
}

#define OW2 12
#define OW4 12
#define XA 2
#define XB 6
template <int G>
__global__ __launch_bounds__(512, 2) void gemm_sk(const char* __restrict__ W0, const char* __restrict__ W1,
                                                  const char* __restrict__ xin, char* __restrict__ ws,
                                                  char* __restrict__ out, const char* __restrict__ pf0,
                                                  const char* __restrict__ pf1) {
  constexpr int N = G == 2 ? 32 : 56, OWN = G == 2 ? OW2 : OW4, S = (N - OWN) / 2, NQ = S + OWN;
  constexpr int RES0 = (S & 3) * STAGE + 8192, RES1 = ((S + 1) & 3) * STAGE + 8192, PT = (NQ & 3) * STAGE + WB;
  constexpr int EX0 = ((NQ - 2) & 3) * STAGE + WB, EX1 = ((NQ - 3) & 3) * STAGE + WB;
  STAMP_DECL
  extern __shared__ __attribute__((aligned(16))) char smem[];
  const u32 lds0 = (u32)(size_t)(LDS_AS char*)smem;
  const int b = blockIdx.x, xcd = b & 7, r = b >> 3, ri = r & 3, rj = r >> 2;
  const int tid = threadIdx.x, lane = tid & 63;
  const int w = __builtin_amdgcn_readfirstlane(tid >> 6);
  const int tx = (xcd & 3) * 4 + ri, tw = (xcd >> 2) * 8 + rj;
  float* aux = (float*)(smem + AUX);
  const rsrc_t rw = mkrsrc(ws);

  if (w >= 4) {
    const int lw = w - 4, c = (ri + (rj >> 1)) & 3, rr = lane >> 3;
    const u32 vx = lane * 16 + lw * 4096;
    const char* X = ws + (G == 2 ? WS_PP : WS_P) + (size_t)tx * N * 16384;
    u32 vw[4];
#pragma unroll
    for (int i = 0; i < 4; i++) {
      const int t = tw ^ (i >> 1), j = i & 1;
      if (G == 2) {
        const int row = t * 64 + (lw >> 1) * 32 + (lw & 1) * 4 + j * 16 + (rr >> 2) * 8 + (rr & 3);
        vw[i] = row * 2048 + (((lane & 7) ^ rr) << 4) - j * 1024;
      } else {
        const int row = (lw * 2 + j) * 8 + rr;
        const int sw = (((row >> 1) & 1) | (((row >> 3) & 1) << 1)) << 1;
        vw[i] = row * 2048 + t * 128 + (((lane & 7) ^ sw) << 4) - j * 1024;
      }
    }
    auto issue = [&](int q, auto sh) {
      constexpr bool SH = decltype(sh)::value;
      const int s = SH ? (tw & 1) * S + kstep<KMS>(q, c, S) : 2 * S + kstep<KMO>(q - S, c, OWN);
      const u32 sb = lds0 + (q & 3) * STAGE;
      const char* wp = G == 2 ? (s < 16 ? W0 : W1) + (s & 15) * 128 : W0 + (size_t)s * (64 * 2048);
      glds2(wp, sb + lw * 2048, vw[0], vw[1]);
      if (SH) glds2(wp, sb + 8192 + lw * 2048, vw[2], vw[3]);
      glds4(X + (size_t)s * 16384, sb + WB + lw * 4096, vx, vx, vx, vx);
    };
    STAMP(1);
    issue(0, IC<1>{});
    issue(1, IC<1>{});
    STAMP(2);
    WAIT_V(8);
    BAR();
    STAMP(3);
    issue(2, IC<1>{});
    const int gw = b * 4 + lw;
    const char* t0 = (gw & 1 ? pf1 : pf0) + (gw >> 1) * 256 + 13 * 131072;
    auto touch = [&](int j) {
      if (G == 2) stouch2(t0 + j * 131072);
    };
    static_assert(G != 2 || 13 + 2 * (NQ - 3) <= 56, "");
    for (int j = 0; j < S - 3; j++) {
      issue(j + 3, IC<1>{});
      touch(j);
      WAIT_V(16);
      BAR();
    }
    issue(S, IC<0>{});
    touch(S - 3);
    WAIT_V(14);
    BAR();
    issue(S + 1, IC<0>{});
    touch(S - 2);
    const u32 rd = lds0 + (lw < 2 ? RES0 : RES1) + (lw & 1) * 4096;
    if (G == 2) {
      const u32 u = (lw * 32 + rr) * 2048 + (((lane & 7) ^ rr) << 4);
      glds4(xin + (size_t)tx * 128 * 2048 + tw * 128, rd, u, u + 16384 - 1024, u + 2 * (16384 - 1024), u + 3 * (16384 - 1024));
    } else {
      glds4(ws + WS_X2 + (size_t)(tx * 16 + tw) * 16384, rd, vx, vx, vx, vx);
    }
    glds_dw(ws + WS_MODF, lds0 + AUX + lw * 256, (lw * 1024 + tw * 64 + lane) * 4);
    WAIT_V(17);
    BAR();
    issue(S + 2, IC<0>{});
    touch(S - 1);
    WAIT_V(17);
    BAR();
#pragma nounroll
    for (int j = S; j < NQ - XB; j++) {
      issue(j + 3, IC<0>{});
      touch(j);
      WAIT_V(12);
      BAR();
    }
    const u32 fl = WS_FL + b * 16 + (G == 2 ? 0 : 8) + (lw >> 1) * 4;
    u32 flag;
    asm volatile("global_load_dword %0, %1, %2 sc1" : "=v"(flag) : "v"(fl), "s"(ws) : "memory");
#pragma unroll
    for (int j = NQ - XB; j < NQ - 3; j++) {
      issue(j + 3, IC<0>{});
      touch(j);
      if (j < NQ - XB + 2 && j < NQ - 4) WAIT_V(13);
      else WAIT_V(12);
      BAR();
    }
    asm volatile("" : "+v"(flag));
    for (int i = 0; __builtin_amdgcn_readfirstlane(flag) == EMPTY && i < SPIN_CAP; i++)
      asm volatile("global_load_dword %0, %1, %2 sc1\n\ts_waitcnt vmcnt(0)" : "=&v"(flag) : "v"(fl), "s"(ws) : "memory");
    glds4_dev(ws + WS_XB + (size_t)b * 16384, lds0 + PT + lw * 4096, vx);
    WAIT_V(10);
    BAR();
    WAIT_V(4);
    BAR();
    WAIT_V(0);
    BAR();
    STAMP(4);
    if (G == 2) {
      const int row = lw * 32 + (lane & 31);
      BAR();
      {
        const f32x2 p = *(const f32x2*)(aux + 2560 + row * 2), q = *(const f32x2*)(aux + 2816 + row * 2);
        const float d = p[0] - q[0];
        u32 m = f2u(0.5f * (p[0] + q[0])), v = f2u(p[1] + q[1] + d * d * 16.f);
        m = m == EMPTY ? 0x7fc00000u : m;
        v = v == EMPTY ? 0x7fc00000u : v;
        if (lane < 32) __builtin_amdgcn_raw_buffer_store_b64(u32x2{m, v}, rw, WS_ST + ((tx * 16 + tw) * 128 + row) * 8, 0, 16);
      }
      const u32 go = WS_ST + ((tx * 16 + (lane >> 5) * 8) * 128 + row) * 8;
      u32 gm[8], gq[8];
#pragma unroll
      for (int i = 0; i < 8; i++) gm[i] = gq[i] = EMPTY;
      const unsigned long long tb = __builtin_amdgcn_s_memrealtime();
      while (true) {
        asm volatile("" ::: "memory");
        u32x2 nv[8];
#pragma unroll
        for (int i = 0; i < 8; i++)
          nv[i] = __builtin_amdgcn_raw_buffer_load_b64(rw, max(gm[i], gq[i]) == EMPTY ? go + i * 1024 : 0x7ffffff0u, 0, 16);
        SB();
        u32 mx = 0;
#pragma unroll
        for (int i = 0; i < 8; i++) {
          const bool miss = max(gm[i], gq[i]) == EMPTY;
          const u32 a0 = nv[i][0], a1 = nv[i][1];
          gm[i] = miss ? a0 : gm[i];
          gq[i] = miss ? a1 : gq[i];
          mx = max(mx, max(gm[i], gq[i]));
        }
        if (!__builtin_amdgcn_ballot_w64(mx == EMPTY) || __builtin_amdgcn_s_memrealtime() - tb > 2000000ull) break;
      }
      float sm = 0.f, q = 0.f;
#pragma unroll
      for (int i = 0; i < 8; i++) sm += u2f(gm[i]);
      sm = sum32(sm) * (1.f / 16);
#pragma unroll
      for (int i = 0; i < 8; i++) {
        const float d = u2f(gm[i]) - sm;
        q += u2f(gq[i]) + 64.f * d * d;
      }
      q = sum32(q);
      if (lane < 32) *(f32x2*)(aux + 3072 + row * 2) = f32x2{sm, __builtin_amdgcn_rsqf(q * (1.f / D_) + 1e-5f)};
      LBAR();
    }
    STAMP_OUT(G, w, 5);
    return;
  }

  TL_DECL
  const int wx = w >> 1, ww = w & 1, g = lane >> 4, cc = lane & 15;
  u32 xa = lds0 + WB + wx * 8192 + lane * 16;
  u32 wa[4];
  if (G == 2) {
#pragma unroll
    for (int i = 0; i < 4; i++)
      wa[i] = lds0 + ww * 8192 + ((i & 1) ^ ww) * 4096 + cc * 128 + ((((i >> 1) * 4 + g) ^ (cc & 7)) << 4);
  } else {
    const int q = cc >> 2, p = lane & 3;
    const int sw = ((q >> 1) | ((g & 1) << 1)) ^ (ww << 1);
#pragma unroll
    for (int n = 0; n < 4; n++) wa[n] = lds0 + ww * 8192 + (g * 8 + q) * 128 + ((n ^ sw) << 5) + p * 8;
  }
  f32x4 acc[4][4];
#pragma unroll
  for (int i = 0; i < 16; i++) {
    acc[i >> 2][i & 3] = f32x4{0.f, 0.f, 0.f, 0.f};
    pin(acc[i >> 2][i & 3]);
  }
  bf16x8 fx[2][4], fw[2][4];
  auto rd = [&](int bf, int kk, u32 so) {
#pragma unroll
    for (int n = 0; n < 4; n++) {
      if (G == 2) fw[bf][n] = lds_b128(wa[kk * 2 + (n >> 1)] + so + (n & 1) * 2048);
      else fw[bf][n] = lds_tr2(wa[n] + so + kk * 4096, wa[n] + so + kk * 4096 + 512);
    }
#pragma unroll
    for (int m = 0; m < 4; m++) fx[bf][m] = lds_b128(xa + so + m * 2048 + kk * 1024);
  };
  auto mm = [&](int bf) {
#pragma unroll
    for (int m = 0; m < 4; m++)
#pragma unroll
      for (int i = 0; i < 4; i++) {
        const int n = (m & 1) ? 3 - i : i;
        acc[m][n] = mfma16(fw[bf][n], fx[bf][m], acc[m][n]);
      }
    sched_rm<G == 2 ? 0 : 4, 16>();
  };
  auto pins = [&]() { pin(xa); pin(wa[0]); pin(wa[1]); pin(wa[2]); pin(wa[3]); };
  auto flip = [&](u32 mx, u32 mw) { xa ^= mx; wa[0] ^= mw; wa[1] ^= mw; wa[2] ^= mw; wa[3] ^= mw; };
  pins();
  asm volatile("" ::"s"(out));
  STAMP(1);
  LBAR(); TL();
  STAMP(2);
  rd(0, 0, 0);
  SB();
  static_assert(S % 2 == 0 && OWN % 2 == 0, "");
  for (int k = S / 2 - 1; k >= 0; k--) {
    WAIT_L(); SB();
    rd(1, 1, 0);
    mm(0);
    LBAR(); TL(); SB();
    rd(0, 0, STAGE);
    mm(1);
    WAIT_L(); SB();
    rd(1, 1, STAGE);
    const u32 t = k ? 0 : ww;
    flip(2 * STAGE | t * 1024, 2 * STAGE | t * (G == 2 ? 8192 + 64 : 8192 + 4096));
    mm(0);
    LBAR(); TL(); SB();
    rd(0, 0, 0);
    mm(1);
  }
  if (ww) {
#pragma unroll
    for (int m = 0; m < 4; m++)
#pragma unroll
      for (int h = 0; h < 2; h++) {
        const f32x4 v = acc[m][2 * h], u = acc[m][2 * h + 1];
        st16(rw, WS_XB + (b ^ 32) * 16384 + wx * 8192 + ((h ^ 1) * 4 + m) * 1024 + lane * 16,
             u32x4{pack2(v[0], v[1]), pack2(v[2], v[3]), pack2(u[0], u[1]), pack2(u[2], u[3])});
        acc[m][2 * h] = acc[m][2 * h + 1] = f32x4{0.f, 0.f, 0.f, 0.f};
        pin(acc[m][2 * h]); pin(acc[m][2 * h + 1]);
      }
  }
  const u32 fl = WS_FL + (b ^ 32) * 16 + (G == 2 ? 0 : 8) + wx * 4;
  for (int i = 0; i < OWN / 2 - 1; i++) {
    LBAR(); TL(); SB();
    rd(1, 0, STAGE);
    flip(2 * STAGE, 2 * STAGE);
    mm(0);
    if ((XA & 1) == 0 && i == XA / 2 && ww) {
      WAIT_V(0);
      if (lane == 0) __builtin_amdgcn_raw_buffer_store_b32(1u, rw, fl, 0, 16);
    }
    LBAR(); TL(); SB();
    rd(0, 0, 0);
    mm(1);
    if ((XA & 1) == 1 && i == XA / 2 && ww) {
      WAIT_V(0);
      if (lane == 0) __builtin_amdgcn_raw_buffer_store_b32(1u, rw, fl, 0, 16);
    }
  }
  LBAR(); TL(); SB();
  rd(1, 0, STAGE);
  mm(0);
  const u32 ex0 = (wx ? EX1 : EX0) + lane * 16;
  WAIT_L(); SB();
#pragma unroll
  for (int i = 0; i < 16; i++) {
    const int n = 3 - (i >> 2), m = (n & 1) ? 3 - (i & 3) : (i & 3);
    acc[m][n] = mfma16(fw[1][n], fx[1][m], acc[m][n]);
    if (i >= 4 && i < 12) {
      const int k = i - 4, n = 3 - (k >> 2), m = (n & 1) ? 3 - (k & 3) : (k & 3);
      *(f32x4*)(smem + ex0 + ww * 8192 + (m * 2 + n - 2) * 1024) = acc[m][n];
    }
    SB();
  }
  LBAR(); TL();
  STAMP(3);
  u32x4 res[4], pt[4];
  f32x4 ex[4][2];
#pragma unroll
  for (int m = 0; m < 4; m++) {
    pt[m] = *(const u32x4*)(smem + PT + w * 4096 + m * 1024 + lane * 16);
#pragma unroll
    for (int j = 0; j < 2; j++) ex[m][j] = *(const f32x4*)(smem + ex0 + (ww ^ 1) * 8192 + (m * 2 + j) * 1024);
    const u32 o = G == 2 ? (m * 16 + cc) * 128 + (((ww * 4 + g) ^ (cc & 7)) << 4)
                         : m * 2048 + (ww * 4 + (g & 1) * 2 + (g >> 1)) * 256 + cc * 16;
    res[m] = *(const u32x4*)(smem + (wx ? RES1 : RES0) + o);
  }
  const int e0 = G == 2 ? ww * 32 + g * 8 : ww * 32 + (g & 1) * 16 + (g >> 1) * 8, go = G == 2 ? 0 : 192;
  const f32x4 g0 = *(const f32x4*)(aux + go + e0), g1 = *(const f32x4*)(aux + go + e0 + 4);
  SB();
#pragma unroll
  for (int m = 0; m < 4; m++)
#pragma unroll
    for (int j = 0; j < 2; j++)
      acc[m][j] += f32x4{bf_lo(pt[m][2 * j]), bf_hi(pt[m][2 * j]), bf_lo(pt[m][2 * j + 1]), bf_hi(pt[m][2 * j + 1])} + ex[m][j];

  if (G == 2) {
    const u32 px = (tx * 16 + tw) * 16384 + wx * 8192 + ww * 1024 + lane * 16;
    u32x4 o[4];
    float f[4][8], mean[4], m2[4];
#pragma unroll
    for (int m = 0; m < 4; m++) {
      const f32x4 a = acc[m][0] * g0, bb = acc[m][1] * g1;
      o[m] = u32x4{pack2(bf_lo(res[m][0]) + a[0], bf_hi(res[m][0]) + a[1]), pack2(bf_lo(res[m][1]) + a[2], bf_hi(res[m][1]) + a[3]),
                   pack2(bf_lo(res[m][2]) + bb[0], bf_hi(res[m][2]) + bb[1]), pack2(bf_lo(res[m][3]) + bb[2], bf_hi(res[m][3]) + bb[3])};
#pragma unroll
      for (int i = 0; i < 4; i++) { f[m][2 * i] = bf_lo(o[m][i]); f[m][2 * i + 1] = bf_hi(o[m][i]); }
      mean[m] = (((f[m][0] + f[m][1]) + (f[m][2] + f[m][3])) + ((f[m][4] + f[m][5]) + (f[m][6] + f[m][7]))) * 0.125f;
      m2[m] = 0.f;
#pragma unroll
      for (int i = 0; i < 8; i++) m2[m] += (f[m][i] - mean[m]) * (f[m][i] - mean[m]);
    }
#pragma unroll
    for (int m = 0; m < 2; m++) {
      auto t = __builtin_amdgcn_permlane32_swap(f2u(mean[m]), f2u(mean[m + 2]), false, false);
      auto u = __builtin_amdgcn_permlane32_swap(f2u(m2[m]), f2u(m2[m + 2]), false, false);
      const float d = u2f(t[0]) - u2f(t[1]);
      mean[m] = 0.5f * (u2f(t[0]) + u2f(t[1]));
      m2[m] = u2f(u[0]) + u2f(u[1]) + d * d * 4.f;
    }
    {
      auto t = __builtin_amdgcn_permlane16_swap(f2u(mean[0]), f2u(mean[1]), false, false);
      auto u = __builtin_amdgcn_permlane16_swap(f2u(m2[0]), f2u(m2[1]), false, false);
      const float d = u2f(t[0]) - u2f(t[1]);
      *(f32x2*)(aux + 2560 + ww * 256 + (wx * 64 + lane) * 2) =
          f32x2{0.5f * (u2f(t[0]) + u2f(t[1])), u2f(u[0]) + u2f(u[1]) + d * d * 8.f};
    }
    LBAR(); TL();
#pragma unroll
    for (int m = 0; m < 4; m++) st16(rw, WS_X2 + px + m * 2048, o[m]);
    const f32x4 s0 = *(const f32x4*)(aux + 64 + e0), s1 = *(const f32x4*)(aux + 64 + e0 + 4);
    const f32x4 c0 = *(const f32x4*)(aux + 128 + e0), c1 = *(const f32x4*)(aux + 128 + e0 + 4);
    STAMP(4);
    LBAR(); TL();
#pragma unroll
    for (int m = 0; m < 4; m++) {
      const f32x2 st = *(const f32x2*)(aux + 3072 + (wx * 64 + m * 16 + cc) * 2);
      float h[8];
#pragma unroll
      for (int i = 0; i < 8; i++) h[i] = (f[m][i] - st[0]) * st[1] * (i < 4 ? c0[i & 3] : c1[i & 3]) + (i < 4 ? s0[i & 3] : s1[i & 3]);
      st16(rw, WS_H2 + px + m * 2048, u32x4{pack2(h[0], h[1]), pack2(h[2], h[3]), pack2(h[4], h[5]), pack2(h[6], h[7])});
    }
  } else {
    const rsrc_t ro = mkrsrc(out, S_ * D_ * 2);
#pragma unroll
    for (int m = 0; m < 4; m++) {
      f32x4 a, bb;
#pragma unroll
      for (int i = 0; i < 4; i++) {
        auto sw = __builtin_amdgcn_permlane16_swap(f2u(acc[m][0][i]), f2u(acc[m][1][i]), false, false);
        a[i] = u2f(sw[0]);
        bb[i] = u2f(sw[1]);
      }
      a *= g0; bb *= g1;
      u32x4 o = {pack2(bf_lo(res[m][0]) + a[0], bf_hi(res[m][0]) + a[1]), pack2(bf_lo(res[m][1]) + a[2], bf_hi(res[m][1]) + a[3]),
                 pack2(bf_lo(res[m][2]) + bb[0], bf_hi(res[m][2]) + bb[1]), pack2(bf_lo(res[m][3]) + bb[2], bf_hi(res[m][3]) + bb[3])};
      st16(ro, (tx * 128 + wx * 64 + m * 16 + cc) * 2048 + (tw * 64 + e0) * 2, o);
    }
  }
  STAMP_OUT(G, w, (G == 2 ? 5 : 4));
  TL_OUT(G, w);
}

constexpr int G3_UP = 14336 + 16, G3_WS = 2 * G3_UP, G3_WOFF = 65536, G3_PF = G3_WOFF + 3 * G3_WS, G3_LDS = G3_PF + 256;
DI float geglu(float gt, float up) {
  const float k1 = -2.302208198f, k3 = -0.1029432397f;
  const float e = __builtin_amdgcn_exp2f(gt * (k1 + k3 * gt * gt));
  return gt * __builtin_amdgcn_rcpf(1.f + e) * up;
}
__global__ __launch_bounds__(512) void gemm_g3(const char* __restrict__ Wg, const char* __restrict__ Wu,
                                               const char* __restrict__ Wd, char* __restrict__ ws) {
  constexpr int N = 16;
  STAMP_DECL
  extern __shared__ __attribute__((aligned(16))) char smem[];
  const u32 lds0 = (u32)(size_t)(LDS_AS char*)smem;
  const int b = blockIdx.x, xcd = b & 7, r = b >> 3, ri = r & 3, rj = r >> 2;
  const int tm = (xcd & 1) * 4 + ri, tn = (xcd >> 1) * 8 + rj, c = (ri + rj) & 3;
  const int tid = threadIdx.x, lane = tid & 63;
  const int w = __builtin_amdgcn_readfirstlane(tid >> 6);
  const int grp = w >> 2, lw = w & 3, wx = lw >> 1, wn = lw & 1, g = lane >> 4, cc = lane & 15;

  TL_DECL
  u32 vo[7];
  const char* gb;
  if (grp == 0) {
    gb = (lw < 2 ? Wg : Wu) + tn * 224;
#pragma unroll
    for (int i = 0; i < 7; i++) {
      const int pi = ((lw & 1) * 7 + i) * 64 + lane, pr = pi / 14, ch = pi - pr * 14;
      const int k = (pr & ~12) | ((pr & 4) << 1) | ((pr & 8) >> 1);
      vo[i] = k * 7168 + ch * 16 - (i & 3) * 1024;
    }
  } else {
    gb = ws + WS_H2 + (size_t)tm * 2 * N * 16384;
#pragma unroll
    for (int i = 0; i < 7; i++) vo[i] = lane * 16 + lw * 4096;
  }
  auto issueW = [&](int q, int slot) {
    const char* p = gb + (size_t)kstep<KM3>(q, c, N) * (64 * 7168);
    const u32 d = lds0 + G3_WOFF + slot * G3_WS + (lw >> 1) * G3_UP + (lw & 1) * 7168;
    glds4(p, d, vo[0], vo[1], vo[2], vo[3]);
    glds3(p, d + 4096, vo[4], vo[5], vo[6]);
  };
  auto issueX = [&](int q, int half) {
    const char* p = gb + (size_t)(half * N + kstep<KM3>(q, c, N)) * 16384;
    glds4(p, lds0 + half * 32768 + (q & 1) * 16384 + lw * 4096, vo[0], vo[0], vo[0], vo[0]);
  };
  const u32 xa = lds0 + grp * 32768 + wx * 8192 + lane * 16;
  const int q4 = cc >> 2, p4 = lane & 3;
  const u32 rowb = lds0 + G3_WOFF + ((g >> 1) * 16 + (g & 1) * 4 + q4) * 224 + (p4 & 1) * 8;
  const u32 wa = rowb + (p4 >> 1) * 16 + wn * 96;
  const u32 wm = rowb + (p4 >> 1) * G3_UP + (12 + wn) * 16;
  f32x4 acc[4][7];
#pragma unroll
  for (int m = 0; m < 4; m++)
#pragma unroll
    for (int n = 0; n < 7; n++) acc[m][n] = f32x4{0.f, 0.f, 0.f, 0.f};
  bf16x8 fx[2][4], fw[2][7];
  auto rd = [&](int k, int slot, auto np, const char* p0, u32 d0, const char* p1, u32 d1) {
    constexpr int NP = decltype(np)::value;
    const u32 xo = xa + (k & 1) * 16384, wo = wa + slot * G3_WS, mo = wm + slot * G3_WS;
    auto piece = [&](auto ic) {
      constexpr int I = decltype(ic)::value;
      if constexpr (I < NP) glds1<(I & 3) * 1024>(I < 4 ? p0 : p1, I < 4 ? d0 : d1, vo[I < 7 ? I : 0]);
    };
    auto rw = [&](int kk, int n) {
      const u32 a = wo + kk * 7168 + (n / 3) * G3_UP + (n % 3) * 32;
      fw[kk][n] = lds_tr2(a, a + 1792);
    };
    auto half = [&](auto kc) {
      constexpr int kk = decltype(kc)::value;
      piece(IC<kk * 4>{});
      fx[kk][0] = lds_b128(xo + kk * 1024); rw(kk, 0); rw(kk, 1);
      piece(IC<kk * 4 + 1>{});
      fx[kk][1] = lds_b128(xo + 2048 + kk * 1024); rw(kk, 2); rw(kk, 3);
      piece(IC<kk * 4 + 2>{});
      fx[kk][2] = lds_b128(xo + 4096 + kk * 1024); rw(kk, 4); rw(kk, 5);
      piece(IC<kk * 4 + 3>{});
      fx[kk][3] = lds_b128(xo + 6144 + kk * 1024);
      fw[kk][6] = lds_tr2(mo + kk * 7168, mo + kk * 7168 + 1792);
    };
    half(IC<0>{});
    half(IC<1>{});
  };
#define EB 1
  auto mm = [&](auto wv) {
    constexpr int WV = decltype(wv)::value;
    __builtin_amdgcn_s_setprio(1);
#pragma unroll
    for (int i = 0; i < 56; i++) {
      const int kk = i / 28, n = (i % 28) / 4, m = (n & 1) ? 3 - (i & 3) : (i & 3);
      if (i == 56 - EB && WV >= 0) {
        SB();
        WAIT_V(WV);
        BAR();
        if (EB > 1) __builtin_amdgcn_s_setprio(3);
        SB();
      }
      acc[m][n] = mfma16(fw[kk][n], fx[kk][m], acc[m][n]);
    }
    __builtin_amdgcn_s_setprio(0);
    SB();
  };
  if (grp == 0) {
    auto wsrc = [&](int q) { return gb + (size_t)kstep<KM3>(q, c, N) * (64 * 7168); };
    const u32 wd = lds0 + G3_WOFF + (lw >> 1) * G3_UP + (lw & 1) * 7168;
    issueW(0, 0);
    STAMP(1);
    WAIT_V(0);
    BAR();
    STAMP(2);
    issueW(1, 1);
    int slot = 0;
    for (int k = 0; k < N - 2; k++) {
      const int s2 = slot == 0 ? 2 : slot - 1;
      const char* p = wsrc(k + 2);
      rd(k, slot, IC<7>{}, p, wd + s2 * G3_WS, p, wd + s2 * G3_WS + 4096);
      if (k == 2 + 3 * lw && lane < 56) glds_dw(Wd, lds0 + G3_PF, ((b * 4 + lw) * 56 + lane) * 128);
      SB(); LBAR(); TL(); SB();
      mm(IC<7>{});
      slot = slot == 2 ? 0 : slot + 1;
    }
    rd(N - 2, (N - 2) % 3, IC<0>{}, gb, 0, gb, 0);
    SB(); LBAR(); TL(); SB();
    mm(IC<0>{});
    rd(N - 1, (N - 1) % 3, IC<0>{}, gb, 0, gb, 0);
    SB(); LBAR(); TL(); SB();
    mm(IC<0>{});
  } else {
    auto xsrc = [&](int q, int half) { return gb + (size_t)(half * N + kstep<KM3>(q, c, N)) * 16384; };
    const u32 xd = lds0 + lw * 4096;
    issueX(0, 0);
    issueX(0, 1);
    STAMP(1);
    WAIT_V(4);
    BAR();
    issueX(1, 0);
    WAIT_V(4);
    BAR();
    STAMP(2);
    int slot = 0;
    for (int k = 0; k < N - 2; k++) {
      rd(k, slot, IC<8>{}, xsrc(k + 1, 1), xd + 32768 + ((k + 1) & 1) * 16384, xsrc(k + 2, 0), xd + (k & 1) * 16384);
      SB();
      WAIT_V(8);
      LBAR(); TL(); SB();
      mm(IC<4>{});
      slot = slot == 2 ? 0 : slot + 1;
    }
    rd(N - 2, (N - 2) % 3, IC<4>{}, xsrc(N - 1, 1), xd + 32768 + ((N - 1) & 1) * 16384, gb, 0);
    SB();
    WAIT_V(4);
    LBAR(); TL(); SB();
    mm(IC<0>{});
    rd(N - 1, (N - 1) % 3, IC<0>{}, gb, 0, gb, 0);
    SB(); LBAR(); TL(); SB();
    mm(IC<-1>{});
  }
  STAMP(3);
  const rsrc_t rw = mkrsrc(ws);
  const u32 pb = WS_P + (tm * 2 + grp) * 56 * 16384 + wx * 8192 + cc * 16;
  auto st = [&](int mt, int chunk, u32x4 v) {
    const int Gs = tn * 14 + chunk;
    st16(rw, pb + (Gs >> 3) * 16384 + mt * 2048 + (Gs & 7) * 256, v);
  };
  u32 t2[4][2];
#pragma unroll
  for (int m = 0; m < 4; m++) {
    u32 rr[3][2];
#pragma unroll
    for (int t = 0; t < 3; t++)
#pragma unroll
      for (int j = 0; j < 2; j++)
        rr[t][j] = pack2(geglu(acc[m][t][2 * j], acc[m][3 + t][2 * j]), geglu(acc[m][t][2 * j + 1], acc[m][3 + t][2 * j + 1]));
    auto s0 = __builtin_amdgcn_permlane16_swap(rr[0][0], rr[1][0], false, false);
    auto s1 = __builtin_amdgcn_permlane16_swap(rr[0][1], rr[1][1], false, false);
    st(m, wn * 6 + (g & 1) * 2 + (g >> 1), u32x4{s0[0], s1[0], s0[1], s1[1]});
    t2[m][0] = rr[2][0];
    t2[m][1] = rr[2][1];
  }
  u32 mx[2][2];
#pragma unroll
  for (int h = 0; h < 2; h++) {
    auto s0 = __builtin_amdgcn_permlane16_swap(t2[2 * h][0], t2[2 * h + 1][0], false, false);
    auto s1 = __builtin_amdgcn_permlane16_swap(t2[2 * h][1], t2[2 * h + 1][1], false, false);
    st(2 * h + (g & 1), wn * 6 + 4 + (g >> 1), u32x4{s0[0], s1[0], s0[1], s1[1]});
    float o[4];
#pragma unroll
    for (int i = 0; i < 4; i++) {
      auto sw = __builtin_amdgcn_permlane32_swap(f2u(acc[2 * h][6][i]), f2u(acc[2 * h + 1][6][i]), false, false);
      o[i] = geglu(u2f(sw[0]), u2f(sw[1]));
    }
    mx[h][0] = pack2(o[0], o[1]);
    mx[h][1] = pack2(o[2], o[3]);
  }
  {
    auto s0 = __builtin_amdgcn_permlane16_swap(mx[0][0], mx[1][0], false, false);
    auto s1 = __builtin_amdgcn_permlane16_swap(mx[0][1], mx[1][1], false, false);
    st((g & 1) * 2 + (g >> 1), 12 + wn, u32x4{s0[0], s1[0], s0[1], s1[1]});
  }
  STAMP_OUT(3, w, 4);
  TL_OUT(3, w);
}

extern "C" void kernel_launch(void* const* d_in, const int* in_sizes, int n_in, void* d_out, int out_size, void* d_ws,
                              size_t ws_size, hipStream_t stream) {
  const char* x = (const char*)d_in[0];
  char* ws = (char*)d_ws;
  (void)hipFuncSetAttribute((const void*)gemm_g1, hipFuncAttributeMaxDynamicSharedMemorySize, PC_LDS);
  (void)hipFuncSetAttribute((const void*)gemm_sk<2>, hipFuncAttributeMaxDynamicSharedMemorySize, PC_LDS);
  (void)hipFuncSetAttribute((const void*)gemm_sk<4>, hipFuncAttributeMaxDynamicSharedMemorySize, PC_LDS);
  (void)hipFuncSetAttribute((const void*)gemm_g3, hipFuncAttributeMaxDynamicSharedMemorySize, G3_LDS);
  k0_prep<<<256, 512, 0, stream>>>((const u16*)x, (const u16*)d_in[9], (const u16*)d_in[10], ws);
  gemm_g1<<<256, 512, PC_LDS, stream>>>((const char*)d_in[1], (const char*)d_in[2], ws, (const char*)d_in[3],
                                        (const char*)d_in[4], (const char*)d_in[6], (const char*)d_in[7]);
  gemm_sk<2><<<256, 512, PC_LDS, stream>>>((const char*)d_in[3], (const char*)d_in[4], x, ws, (char*)d_out,
                                           (const char*)d_in[6], (const char*)d_in[7]);
  gemm_g3<<<256, 512, G3_LDS, stream>>>((const char*)d_in[6], (const char*)d_in[7], (const char*)d_in[8], ws);
  gemm_sk<4><<<256, 512, PC_LDS, stream>>>((const char*)d_in[8], (const char*)d_in[8], x, ws, (char*)d_out, x, x);
}
```

```cpp
#include <hip/hip_runtime.h>
#include <hip/hip_bf16.h>

typedef unsigned short u16;
typedef unsigned int u32;
typedef __bf16 bf16x8 __attribute__((ext_vector_type(8)));
typedef __bf16 bf16x2 __attribute__((ext_vector_type(2)));
typedef short s16x4 __attribute__((ext_vector_type(4)));
typedef float f32x4 __attribute__((ext_vector_type(4)));
typedef float f32x2 __attribute__((ext_vector_type(2)));
typedef u32 u32x4 __attribute__((ext_vector_type(4)));
typedef u32 u32x2 __attribute__((ext_vector_type(2)));
#define LDS_AS __attribute__((address_space(3)))
#define DI __device__ __forceinline__

constexpr int S_ = 2048, D_ = 1024, F_ = 3584;
constexpr int KS = 8;

constexpr size_t WS_MODP = 0;
constexpr size_t WS_ST = 262144;
constexpr size_t WS_FL = 524288;
constexpr size_t WS_MODF = 540672;
constexpr size_t WS_NT = 1u << 20;
constexpr size_t WS_PP = WS_NT + (4u << 20);
constexpr size_t WS_X2 = WS_PP + (8u << 20);
constexpr size_t WS_H2 = WS_X2 + (4u << 20);
constexpr size_t WS_P = WS_H2 + (4u << 20);
constexpr size_t WS_XB = WS_P + (14u << 20);

DI float bf_lo(u32 v) { return __builtin_bit_cast(float, v << 16); }
DI float bf_hi(u32 v) { return __builtin_bit_cast(float, v & 0xffff0000u); }
DI u32 pack2(float a, float b) {
  f32x2 v = {a, b};
  return __builtin_bit_cast(u32, __builtin_convertvector(v, bf16x2));
}
DI u32 f2u(float a) { return __builtin_bit_cast(u32, a); }
DI float u2f(u32 a) { return __builtin_bit_cast(float, a); }
DI float rbf(float a) { return bf_lo(pack2(a, a)); }
DI float wave_sum(float v) {
#pragma unroll
  for (int o = 32; o; o >>= 1) v += __shfl_xor(v, o, 64);
  return v;
}
DI float sum32(float v) {
  auto t = __builtin_amdgcn_permlane32_swap(f2u(v), f2u(v), false, false);
  return u2f(t[0]) + u2f(t[1]);
}
DI f32x4 mfma16(bf16x8 a, bf16x8 b, f32x4 c) { return __builtin_amdgcn_mfma_f32_16x16x32_bf16(a, b, c, 0, 0, 0); }
DI bf16x8 lds_b128(u32 addr) { return *(const LDS_AS bf16x8*)(size_t)addr; }
DI bf16x8 lds_tr2(u32 a0, u32 a1) {
  s16x4 lo = __builtin_amdgcn_ds_read_tr16_b64_v4i16((LDS_AS s16x4*)(size_t)a0);
  s16x4 hi = __builtin_amdgcn_ds_read_tr16_b64_v4i16((LDS_AS s16x4*)(size_t)a1);
  u32x2 l = __builtin_bit_cast(u32x2, lo), h = __builtin_bit_cast(u32x2, hi);
  u32x4 r = {l[0], l[1], h[0], h[1]};
  return __builtin_bit_cast(bf16x8, r);
}
#define WAIT_V(n) asm volatile("s_waitcnt vmcnt(%0)" ::"i"(n) : "memory")
#define BAR() asm volatile("s_barrier" ::: "memory")
#define WAIT_L() __builtin_amdgcn_s_waitcnt(0xC07F)
#define LBAR() do { asm volatile("" ::: "memory"); WAIT_L(); BAR(); } while (0)

#define STAMP_DECL
#define STAMP(i)
#define STAMP_OUT(kid, wv, n)

#define TL_DECL
#define TL()
#define TL_OUT(kid, wv)

DI void glds4(const char* sbase, u32 m0v, u32 v0, u32 v1, u32 v2, u32 v3) {
  asm volatile(
      "s_mov_b32 m0, %1\n\ts_nop 0\n\t"
      "global_load_lds_dwordx4 %2, %0\n\t"
      "global_load_lds_dwordx4 %3, %0 offset:1024\n\t"
      "global_load_lds_dwordx4 %4, %0 offset:2048\n\t"
      "global_load_lds_dwordx4 %5, %0 offset:3072" ::"s"(sbase),
      "s"(m0v), "v"(v0), "v"(v1), "v"(v2), "v"(v3)
      : "memory");
}
DI void glds3(const char* sbase, u32 m0v, u32 v0, u32 v1, u32 v2) {
  asm volatile(
      "s_mov_b32 m0, %1\n\ts_nop 0\n\t"
      "global_load_lds_dwordx4 %2, %0\n\t"
      "global_load_lds_dwordx4 %3, %0 offset:1024\n\t"
      "global_load_lds_dwordx4 %4, %0 offset:2048" ::"s"(sbase),
      "s"(m0v), "v"(v0), "v"(v1), "v"(v2)
      : "memory");
}
DI void glds2(const char* sbase, u32 m0v, u32 v0, u32 v1) {
  asm volatile(
      "s_mov_b32 m0, %1\n\ts_nop 0\n\t"
      "global_load_lds_dwordx4 %2, %0\n\t"
      "global_load_lds_dwordx4 %3, %0 offset:1024" ::"s"(sbase),
      "s"(m0v), "v"(v0), "v"(v1)
      : "memory");
}
template <int OFF>
DI void glds1(const char* sbase, u32 m0v, u32 v0) {
  asm volatile("s_mov_b32 m0, %1\n\ts_nop 0\n\tglobal_load_lds_dwordx4 %2, %0 offset:%3" ::"s"(sbase), "s"(m0v), "v"(v0),
               "i"(OFF)
               : "memory");
}
DI void glds4_dev(const char* sbase, u32 m0v, u32 v0) {
  asm volatile(
      "s_mov_b32 m0, %1\n\ts_nop 0\n\t"
      "global_load_lds_dwordx4 %2, %0 sc1\n\t"
      "global_load_lds_dwordx4 %2, %0 offset:1024 sc1\n\t"
      "global_load_lds_dwordx4 %2, %0 offset:2048 sc1\n\t"
      "global_load_lds_dwordx4 %2, %0 offset:3072 sc1" ::"s"(sbase),
      "s"(m0v), "v"(v0)
      : "memory");
}
DI void glds_dw(const char* sbase, u32 m0v, u32 v0) {
  asm volatile("s_mov_b32 m0, %1\n\ts_nop 0\n\tglobal_load_lds_dword %2, %0" ::"s"(sbase), "s"(m0v), "v"(v0) : "memory");
}
DI void glds_dw4(const char* sbase, u32 m0v, u32 v0, u32 v1, u32 v2, u32 v3) {
  asm volatile(
      "s_mov_b32 m0, %1\n\ts_nop 0\n\t"
      "global_load_lds_dword %2, %0\n\t"
      "global_load_lds_dword %3, %0 offset:256\n\t"
      "global_load_lds_dword %4, %0 offset:512\n\t"
      "global_load_lds_dword %5, %0 offset:768" ::"s"(sbase),
      "s"(m0v), "v"(v0), "v"(v1), "v"(v2), "v"(v3)
      : "memory");
}

typedef __amdgpu_buffer_rsrc_t rsrc_t;
DI rsrc_t mkrsrc(const void* p, int bytes = 0x4000000) { return __builtin_amdgcn_make_buffer_rsrc((void*)p, 0, bytes, 0x00020000); }
template <int AUXV = 16>
DI void st16(rsrc_t r, u32 off, u32x4 v) { __builtin_amdgcn_raw_buffer_store_b128(v, r, off, 0, AUXV); }
#define SP0 16
#define SPG1 16
#define SPX2 16
#define SPH2 16
#define SPG3 16
#define SPG4 16
DI void stouch2(const char* p) {
  asm volatile("s_load_dwordx2 s[94:95], %0, 60\n\ts_load_dwordx2 s[94:95], %0, 188" ::"s"(p) : "s94", "s95");
}
DI int kstep0(int q, int c, int n) {
  if (q == 0) return c;
  int t = q - 1;
  if (t + 4 >= n + ((t - c) & 3) && t >= c) return q;
  return ((t & 3) == c) ? t + 4 : t;
}
template <int KM>
DI int kstep(int q, int c, int n) {
  if (KM == 0) return kstep0(q, c, n);
  if (KM == 9) return q;
  if (KM >= 10) {
    constexpr int L = KM % 10, F = KM / 10;
    const int d = (q + L - c) & 3, s = q + L - d;
    if (s < F || s >= n || d > L) return q;
    return d == 0 ? s : q - 1;
  }
  return q < KM ? q : KM + kstep0(q - KM, c, n - KM);
}
#define KMP 1
#define KMS 43
#define KMO 22
#define KMS4 KMS
#define KMO4 KMO
#define KM3 31

constexpr u32 EMPTY = 0xffffffffu;
constexpr int SPIN_CAP = 1 << 16;
__global__ __launch_bounds__(512) void k0_prep(const u16* __restrict__ x, const u16* __restrict__ cvec,
                                               const u16* __restrict__ wmod, char* __restrict__ ws) {
  __shared__ __attribute__((aligned(16))) u16 sm[8 * 1032];
  __shared__ float sf[8 * 192];
  STAMP_DECL
  const int b = blockIdx.x, tid = threadIdx.x, lane = tid & 63, w = tid >> 6;
  const rsrc_t rw = mkrsrc(ws);
  const u32x4* px = (const u32x4*)(x + (size_t)(b * 8 + w) * D_);
  const u32x4 v0 = px[lane], v1 = px[64 + lane];
  const int ks = b >> 5, cb = b & 31, r0 = ks * 128 + w * 16 + (lane >> 3);
  const u32 c0 = cvec[r0], c1 = cvec[r0 + 8];
  u32x4 wv[6];
#pragma unroll
  for (int i = 0; i < 6; i++)
    wv[i] = *(const u32x4*)(wmod + (size_t)(r0 + (i / 3) * 8) * 6144 + cb * 192 + (i % 3) * 64 + (lane & 7) * 8);
  __builtin_amdgcn_sched_barrier(0);
  {
    float f[16];
#pragma unroll
    for (int i = 0; i < 4; i++) {
      f[2 * i] = bf_lo(v0[i]); f[2 * i + 1] = bf_hi(v0[i]);
      f[8 + 2 * i] = bf_lo(v1[i]); f[8 + 2 * i + 1] = bf_hi(v1[i]);
    }
    float s = 0.f;
#pragma unroll
    for (int i = 0; i < 16; i++) s += f[i];
    const float m = wave_sum(s) * (1.f / D_);
    float q = 0.f;
#pragma unroll
    for (int i = 0; i < 16; i++) { f[i] -= m; q += f[i] * f[i]; }
    const float r = 1.f / sqrtf(wave_sum(q) * (1.f / D_) + 1e-5f);
    u32x4 o0, o1;
#pragma unroll
    for (int i = 0; i < 4; i++) {
      o0[i] = pack2(f[2 * i] * r, f[2 * i + 1] * r);
      o1[i] = pack2(f[8 + 2 * i] * r, f[8 + 2 * i + 1] * r);
    }
    *(u32x4*)(sm + w * 1032 + lane * 8) = o0;
    *(u32x4*)(sm + w * 1032 + 512 + lane * 8) = o1;
    LBAR();
    const int s0 = b * 8;
#pragma unroll
    for (int h = 0; h < 2; h++) {
      const int rho = tid + 512 * h;
      const int d = (rho & ~31) | (((rho >> 2) & 3) << 3) | (((rho >> 4) & 1) << 2) | (rho & 3);
      u32x4 o;
#pragma unroll
      for (int j = 0; j < 4; j++) o[j] = (u32)sm[(2 * j) * 1032 + d] | ((u32)sm[(2 * j + 1) * 1032 + d] << 16);
      const u32 off = ((rho >> 7) * (S_ / 64) + (s0 >> 6)) * 16384 + ((rho >> 4) & 7) * 2048 + ((s0 >> 3) & 7) * 256 + (rho & 15) * 16;
      st16<SP0>(rw, WS_NT + off, o);
    }
  }
  __builtin_amdgcn_sched_barrier(0);
  STAMP(1);
  {
    const float f0 = bf_lo(c0), f1 = bf_lo(c1);
    const float s0 = rbf(f0 / (1.f + __expf(-f0))), s1 = rbf(f1 / (1.f + __expf(-f1)));
    float a[24];
#pragma unroll
    for (int j = 0; j < 3; j++)
#pragma unroll
      for (int i = 0; i < 4; i++) {
        a[j * 8 + 2 * i] = s0 * bf_lo(wv[j][i]) + s1 * bf_lo(wv[3 + j][i]);
        a[j * 8 + 2 * i + 1] = s0 * bf_hi(wv[j][i]) + s1 * bf_hi(wv[3 + j][i]);
      }
#pragma unroll
    for (int i = 0; i < 12; i++) {
      auto t = __builtin_amdgcn_permlane32_swap(f2u(a[i]), f2u(a[i + 12]), false, false);
      a[i] = u2f(t[0]) + u2f(t[1]);
    }
#pragma unroll
    for (int i = 0; i < 6; i++) {
      auto t = __builtin_amdgcn_permlane16_swap(f2u(a[i]), f2u(a[i + 6]), false, false);
      a[i] = u2f(t[0]) + u2f(t[1]);
      a[i] += u2f(__builtin_amdgcn_update_dpp(0u, f2u(a[i]), 0x128, 0xf, 0xf, true));
    }
    if (!(lane & 8)) {
#pragma unroll
      for (int i = 0; i < 6; i++) {
        const int idx = i + 6 * (lane >> 4);
        sf[w * 192 + (idx >> 3) * 64 + (lane & 7) * 8 + (idx & 7)] = a[i];
      }
    }
    if (w == 7) st16(rw, WS_ST + b * 1024 + lane * 16, u32x4{EMPTY, EMPTY, EMPTY, EMPTY});
    if (tid == 384) st16(rw, WS_FL + b * 16, u32x4{EMPTY, EMPTY, EMPTY, EMPTY});
    LBAR();
    if (tid < 192) {
      float s = 0.f;
#pragma unroll
      for (int i = 0; i < 8; i++) s += sf[i * 192 + tid];
      __builtin_amdgcn_raw_buffer_store_b32(f2u(s), rw, WS_MODP + (ks * 6144 + cb * 192 + tid) * 4, 0, 16);
    }
  }
  STAMP_OUT(0, w, 2);
}

constexpr int STAGE = 32768, WB = 16384, AUX = 4 * STAGE;
constexpr int PC_LDS = AUX + 13312;
template <int V> struct IC { static constexpr int value = V; };
#define SB() __builtin_amdgcn_sched_barrier(0)
template <int N2, int NM>
DI void sched_rm() {
#pragma unroll
  for (int i = 0; i < NM; i++) {
    __builtin_amdgcn_sched_group_barrier(0x008, 1, 0);
    if (i < N2) __builtin_amdgcn_sched_group_barrier(0x100, 2, 0);
    else if (i < 8) __builtin_amdgcn_sched_group_barrier(0x100, 1, 0);
    else __builtin_amdgcn_sched_group_barrier(0x006, 2, 0);
  }
  SB();
}
DI void pin(u32& v) { asm volatile("" : "+v"(v)); }
DI void pin(f32x4& v) { asm volatile("" : "+v"(v)); }
DI void mod_load(const char* ws, u32 lds0, int lw, int col) {
  const u32 v = col * 4;
#pragma unroll
  for (int h = 0; h < 2; h++)
    glds_dw4(ws + WS_MODP + h * 4 * 24576, lds0 + AUX + 2048 + lw * 2048 + h * 1024, v, v + 24576 - 256,
             v + 2 * (24576 - 256), v + 3 * (24576 - 256));
}
DI void mod_final(float* aux, int lw, int lane) {
  const float* raw = aux + 512 + lw * 512 + lane;
  const float v = rbf(((raw[0] + raw[64]) + (raw[128] + raw[192])) + ((raw[256] + raw[320]) + (raw[384] + raw[448])));
  aux[lw * 64 + lane] = lw < 2 ? v : rbf(1.f + v);
}

__global__ __launch_bounds__(512, 2) void gemm_g1(const char* __restrict__ W0, const char* __restrict__ W1,
                                                  char* __restrict__ ws, const char* __restrict__ pf0,
                                                  const char* __restrict__ pf1, const char* __restrict__ pf2,
                                                  const char* __restrict__ pf3) {
  constexpr int N = 32;
  STAMP_DECL
  extern __shared__ __attribute__((aligned(16))) char smem[];
  const u32 lds0 = (u32)(size_t)(LDS_AS char*)smem;
  const int b = blockIdx.x, xcd = b & 7, r = b >> 3, ri = r & 3, rj = r >> 2;
  const int tid = threadIdx.x, lane = tid & 63;
  const int w = __builtin_amdgcn_readfirstlane(tid >> 6);
  const int tw = xcd * 4 + ri, tx = rj;
  float* aux = (float*)(smem + AUX);

  if (w >= 4) {
    const int lw = w - 4, c = (ri + rj) & 3, rr = lane >> 3;
    const u32 vx = lane * 16 + lw * 4096;
    const char* X = ws + WS_NT + (size_t)tx * N * 16384;
    const char* Wb = (tw < 16 ? W0 : W1) + (size_t)(tw & 15) * 128 * 4096;
    u32 vw[4];
#pragma unroll
    for (int i = 0; i < 4; i++) vw[i] = ((lw * 4 + i) * 8 + rr) * 4096 + (((lane & 7) ^ rr) << 4) - i * 1024;
    auto issue = [&](int q) {
      const int s = kstep<KMP>(q, c, N);
      const u32 sb = lds0 + (q & 3) * STAGE + lw * 4096;
      glds4(Wb + s * 128, sb, vw[0], vw[1], vw[2], vw[3]);
      glds4(X + (size_t)s * 16384, sb + WB, vx, vx, vx, vx);
    };
    STAMP(1);
    issue(0);
    issue(1);
    STAMP(2);
    WAIT_V(8);
    BAR();
    STAMP(3);
    issue(2);
    const int gw = b * 4 + lw;
    const char* t0 = (gw & 1 ? pf1 : pf0) + (gw >> 1) * 256;
    const char* t1 = (gw & 1 ? pf3 : pf2) + (gw >> 1) * 256 - 16 * 131072;
    for (int j = 0; j < N - 3; j++) {
      issue(j + 3);
      stouch2((j < 16 ? t0 : t1) + j * 131072);
      WAIT_V(16);
      BAR();
    }
    mod_load(ws, lds0, lw, (lw >> 1) * 1024 + tx * 128 + (lw & 1) * 64 + lane);
    WAIT_V(16);
    BAR();
    WAIT_V(8);
    BAR();
    WAIT_V(0);
    const int col = gw * 4 + (lane & 3);
    float v = *(const float*)(ws + WS_MODP + ((lane >> 3) * 6144 + 2048 + col) * 4);
    mod_final(aux, lw, lane);
    BAR();
    LBAR();
    v += u2f(__builtin_amdgcn_update_dpp(0u, f2u(v), 0x128, 0xf, 0xf, true));
    auto t = __builtin_amdgcn_permlane16_swap(f2u(v), f2u(v), false, false);
    v = rbf(sum32(u2f(t[0]) + u2f(t[1])));
    if ((col >> 10) == 2) v = rbf(1.f + v);
    if (lane < 4) __builtin_amdgcn_raw_buffer_store_b32(f2u(v), mkrsrc(ws), WS_MODF + col * 4, 0, 16);
    STAMP_OUT(1, w, 4);
    return;
  }

  TL_DECL
  const int wx = w >> 1, ww = w & 1, g = lane >> 4, cc = lane & 15;
  u32 xa = lds0 + WB + wx * 8192 + lane * 16, wa[4];
#pragma unroll
  for (int i = 0; i < 4; i++)
    wa[i] = lds0 + ww * 8192 + ((i & 1) ^ wx) * 4096 + cc * 128 + ((((i >> 1) * 4 + g) ^ (cc & 7)) << 4);
  f32x4 acc[4][4], rsum[2];
#pragma unroll
  for (int i = 0; i < 18; i++) {
    f32x4& v = i < 16 ? acc[i >> 2][i & 3] : rsum[i & 1];
    v = f32x4{0.f, 0.f, 0.f, 0.f};
    pin(v);
  }
  bf16x8 fx[2][4], fw[2][4];
  auto rd = [&](int kk, u32 so) {
#pragma unroll
    for (int n = 0; n < 4; n++) fw[kk][n] = lds_b128(wa[kk * 2 + (n >> 1)] + so + (n & 1) * 2048);
#pragma unroll
    for (int m = 0; m < 4; m++) fx[kk][m] = lds_b128(xa + so + m * 2048 + kk * 1024);
  };
  const bf16x8 ones = __builtin_bit_cast(bf16x8, u32x4{0x3f803f80u, 0x3f803f80u, 0x3f803f80u, 0x3f803f80u});
  auto mm = [&](int kk) {
#pragma unroll
    for (int m = 0; m < 4; m++)
#pragma unroll
      for (int i = 0; i < 4; i++) {
        const int n = (m & 1) ? 3 - i : i;
        acc[m][n] = mfma16(fx[kk][m], fw[kk][n], acc[m][n]);
      }
    rsum[0] = mfma16(ones, fw[kk][0], rsum[0]);
    rsum[1] = mfma16(ones, fw[kk][1], rsum[1]);
    sched_rm<0, 18>();
  };
  pin(xa); pin(wa[0]); pin(wa[1]); pin(wa[2]); pin(wa[3]);
  STAMP(1);
  LBAR(); TL();
  STAMP(2);
  rd(0, 0);
  SB();
  for (int k = 0; k < N; k += 2) {
    WAIT_L(); SB();
    rd(1, 0);
    mm(0);
    LBAR(); TL(); SB();
    rd(0, STAGE);
    mm(1);
    WAIT_L(); SB();
    rd(1, STAGE);
    xa ^= 2 * STAGE; wa[0] ^= 2 * STAGE; wa[1] ^= 2 * STAGE; wa[2] ^= 2 * STAGE; wa[3] ^= 2 * STAGE;
    mm(0);
    LBAR(); TL(); SB();
    rd(0, 0);
    mm(1);
  }
  if (lane < 32) aux[256 + ww * 64 + wx * 32 + lane] = lane < 16 ? rsum[0][0] : rsum[1][0];
  SB(); LBAR(); TL();
  STAMP(3);
  const rsrc_t rw = mkrsrc(ws);
  const float sg = tw < 16 ? 1.f : -1.f;
  const u32 pp = WS_PP + ((tw & 15) * 32 + (tw >> 4) * 16 + tx * 2 + wx) * 16384 + lane * 16;
#pragma unroll
  for (int h = 0; h < 2; h++) {
    const int d0 = wx * 64 + h * 32 + g * 8;
    const f32x4 s0 = *(const f32x4*)(aux + d0) * sg, s1 = *(const f32x4*)(aux + d0 + 4) * sg;
    const f32x4 c0 = *(const f32x4*)(aux + 128 + d0) * sg, c1 = *(const f32x4*)(aux + 128 + d0 + 4) * sg;
#pragma unroll
    for (int n = 0; n < 4; n++) {
      const int t = ww * 4 + (n ^ (wx * 2));
      const float rsv = aux[256 + t * 16 + cc];
      const f32x4 a = acc[2 * h][n] * c0 + s0 * rsv, bb = acc[2 * h + 1][n] * c1 + s1 * rsv;
      u32x4 o = {pack2(a[0], a[1]), pack2(a[2], a[3]), pack2(bb[0], bb[1]), pack2(bb[2], bb[3])};
      st16<SPG1>(rw, pp + t * 2048 + h * 1024, o);
    }
  }
  STAMP_OUT(1, w, 4);
  TL_OUT(1, w);
}

#define OW2 12
#define OW4 12
#define MAP 1
#define XA 2
#define XB 6
template <int G>
__global__ __launch_bounds__(512, 2) void gemm_sk(const char* __restrict__ W0, const char* __restrict__ W1,
                                                  const char* __restrict__ xin, char* __restrict__ ws,
                                                  char* __restrict__ out, const char* __restrict__ pf0,
                                                  const char* __restrict__ pf1) {
  constexpr int N = G == 2 ? 32 : 56, OWN = G == 2 ? OW2 : OW4, S = (N - OWN) / 2, NQ = S + OWN;
  constexpr int RES0 = (S & 3) * STAGE + 8192, RES1 = ((S + 1) & 3) * STAGE + 8192, PT = (NQ & 3) * STAGE + WB;
  constexpr int EX0 = ((NQ - 2) & 3) * STAGE + WB, EX1 = ((NQ - 3) & 3) * STAGE + WB;
  STAMP_DECL
  extern __shared__ __attribute__((aligned(16))) char smem[];
  const u32 lds0 = (u32)(size_t)(LDS_AS char*)smem;
  const int b = blockIdx.x, xcd = b & 7, r = b >> 3, ri = r & 3, rj = r >> 2;
  const int tid = threadIdx.x, lane = tid & 63;
  const int w = __builtin_amdgcn_readfirstlane(tid >> 6);
  const int tx = (xcd & 3) * 4 + ri, tw = rj * 2 + (xcd >> 2), pb = b ^ 4;
  float* aux = (float*)(smem + AUX);
  const rsrc_t rw = mkrsrc(ws);

  if (w >= 4) {
    const int lw = w - 4, c = (ri + (MAP ? rj : rj >> 1)) & 3, rr = lane >> 3;
    const u32 vx = lane * 16 + lw * 4096;
    const char* X = ws + (G == 2 ? WS_PP : WS_P) + (size_t)tx * N * 16384;
    u32 vw[4];
#pragma unroll
    for (int i = 0; i < 4; i++) {
      const int t = tw ^ (i >> 1), j = i & 1;
      if (G == 2) {
        const int row = t * 64 + (lw >> 1) * 32 + (lw & 1) * 4 + j * 16 + (rr >> 2) * 8 + (rr & 3);
        vw[i] = row * 2048 + (((lane & 7) ^ rr) << 4) - j * 1024;
      } else {
        const int row = (lw * 2 + j) * 8 + rr;
        const int sw = (((row >> 1) & 1) | (((row >> 3) & 1) << 1)) << 1;
        vw[i] = row * 2048 + t * 128 + (((lane & 7) ^ sw) << 4) - j * 1024;
      }
    }
    auto issue = [&](int q, auto sh) {
      constexpr bool SH = decltype(sh)::value;
      int s = SH ? kstep<G == 2 ? KMS : KMS4>(q, c, S) : S + kstep<G == 2 ? KMO : KMO4>(q - S, c, OWN);
      s = s < N / 2 ? s + (tw & 1) * (N / 2) : s - OWN / 2 + (~tw & 1) * (N / 2);
      const u32 sb = lds0 + (q & 3) * STAGE;
      const char* wp = G == 2 ? (s < 16 ? W0 : W1) + (s & 15) * 128 : W0 + (size_t)s * (64 * 2048);
      glds2(wp, sb + lw * 2048, vw[0], vw[1]);
      if (SH) glds2(wp, sb + 8192 + lw * 2048, vw[2], vw[3]);
      glds4(X + (size_t)s * 16384, sb + WB + lw * 4096, vx, vx, vx, vx);
    };
    STAMP(1);
    issue(0, IC<1>{});
    issue(1, IC<1>{});
    STAMP(2);
    WAIT_V(8);
    BAR();
    STAMP(3);
    issue(2, IC<1>{});
    const int gw = b * 4 + lw;
    const char* t0 = (gw & 1 ? pf1 : pf0) + (gw >> 1) * 256 + 13 * 131072;
    auto touch = [&](int j) {
      if (G == 2) stouch2(t0 + j * 131072);
    };
    static_assert(G != 2 || 13 + 2 * (NQ - 3) <= 56, "");
    for (int j = 0; j < S - 3; j++) {
      issue(j + 3, IC<1>{});
      touch(j);
      WAIT_V(16);
      BAR();
    }
    issue(S, IC<0>{});
    touch(S - 3);
    WAIT_V(14);
    BAR();
    issue(S + 1, IC<0>{});
    touch(S - 2);
    const u32 rd = lds0 + (lw < 2 ? RES0 : RES1) + (lw & 1) * 4096;
    if (G == 2) {
      const u32 u = (lw * 32 + rr) * 2048 + (((lane & 7) ^ rr) << 4);
      glds4(xin + (size_t)tx * 128 * 2048 + tw * 128, rd, u, u + 16384 - 1024, u + 2 * (16384 - 1024), u + 3 * (16384 - 1024));
    } else {
      glds4(ws + WS_X2 + (size_t)(tx * 16 + tw) * 16384, rd, vx, vx, vx, vx);
    }
    glds_dw(ws + WS_MODF, lds0 + AUX + lw * 256, (lw * 1024 + tw * 64 + lane) * 4);
    WAIT_V(17);
    BAR();
    issue(S + 2, IC<0>{});
    touch(S - 1);
    WAIT_V(17);
    BAR();
#pragma nounroll
    for (int j = S; j < NQ - XB; j++) {
      issue(j + 3, IC<0>{});
      touch(j);
      WAIT_V(12);
      BAR();
    }
    const u32 fl = WS_FL + b * 16 + (G == 2 ? 0 : 8) + (lw >> 1) * 4;
    u32 flag;
    asm volatile("global_load_dword %0, %1, %2 sc1" : "=v"(flag) : "v"(fl), "s"(ws) : "memory");
#pragma unroll
    for (int j = NQ - XB; j < NQ - 3; j++) {
      issue(j + 3, IC<0>{});
      touch(j);
      if (j < NQ - XB + 2 && j < NQ - 4) WAIT_V(13);
      else WAIT_V(12);
      BAR();
    }
    asm volatile("" : "+v"(flag));
    for (int i = 0; __builtin_amdgcn_readfirstlane(flag) == EMPTY && i < SPIN_CAP; i++)
      asm volatile("global_load_dword %0, %1, %2 sc1\n\ts_waitcnt vmcnt(0)" : "=&v"(flag) : "v"(fl), "s"(ws) : "memory");
    glds4_dev(ws + WS_XB + (G == 2 ? 0 : 4u << 20) + (size_t)b * 16384, lds0 + PT + lw * 4096, vx);
    WAIT_V(10);
    BAR();
    WAIT_V(4);
    BAR();
    WAIT_V(0);
    BAR();
    STAMP(4);
    if (G == 2) {
      const int row = lw * 32 + (lane & 31);
      BAR();
      {
        const f32x2 p = *(const f32x2*)(aux + 2560 + row * 2), q = *(const f32x2*)(aux + 2816 + row * 2);
        const float d = p[0] - q[0];
        u32 m = f2u(0.5f * (p[0] + q[0])), v = f2u(p[1] + q[1] + d * d * 16.f);
        m = m == EMPTY ? 0x7fc00000u : m;
        v = v == EMPTY ? 0x7fc00000u : v;
        if (lane < 32) __builtin_amdgcn_raw_buffer_store_b64(u32x2{m, v}, rw, WS_ST + ((tx * 16 + tw) * 128 + row) * 8, 0, 16);
      }
      const u32 go = WS_ST + ((tx * 16 + (lane >> 5) * 8) * 128 + row) * 8;
      u32 gm[8], gq[8];
#pragma unroll
      for (int i = 0; i < 8; i++) gm[i] = gq[i] = EMPTY;
      const unsigned long long tb = __builtin_amdgcn_s_memrealtime();
      while (true) {
        asm volatile("" ::: "memory");
        u32x2 nv[8];
#pragma unroll
        for (int i = 0; i < 8; i++)
          nv[i] = __builtin_amdgcn_raw_buffer_load_b64(rw, max(gm[i], gq[i]) == EMPTY ? go + i * 1024 : 0x7ffffff0u, 0, 16);
        SB();
        u32 mx = 0;
#pragma unroll
        for (int i = 0; i < 8; i++) {
          const bool miss = max(gm[i], gq[i]) == EMPTY;
          const u32 a0 = nv[i][0], a1 = nv[i][1];
          gm[i] = miss ? a0 : gm[i];
          gq[i] = miss ? a1 : gq[i];
          mx = max(mx, max(gm[i], gq[i]));
        }
        if (!__builtin_amdgcn_ballot_w64(mx == EMPTY) || __builtin_amdgcn_s_memrealtime() - tb > 2000000ull) break;
      }
      float sm = 0.f, q = 0.f;
#pragma unroll
      for (int i = 0; i < 8; i++) sm += u2f(gm[i]);
      sm = sum32(sm) * (1.f / 16);
#pragma unroll
      for (int i = 0; i < 8; i++) {
        const float d = u2f(gm[i]) - sm;
        q += u2f(gq[i]) + 64.f * d * d;
      }
      q = sum32(q);
      if (lane < 32) *(f32x2*)(aux + 3072 + row * 2) = f32x2{sm, __builtin_amdgcn_rsqf(q * (1.f / D_) + 1e-5f)};
      LBAR();
    }
    STAMP_OUT(G, w, 5);
    return;
  }

  TL_DECL
  const int wx = w >> 1, ww = w & 1, g = lane >> 4, cc = lane & 15;
  u32 xa = lds0 + WB + wx * 8192 + lane * 16;
  u32 wa[4];
  if (G == 2) {
#pragma unroll
    for (int i = 0; i < 4; i++)
      wa[i] = lds0 + ww * 8192 + ((i & 1) ^ ww) * 4096 + cc * 128 + ((((i >> 1) * 4 + g) ^ (cc & 7)) << 4);
  } else {
    const int q = cc >> 2, p = lane & 3;
    const int sw = ((q >> 1) | ((g & 1) << 1)) ^ (ww << 1);
#pragma unroll
    for (int n = 0; n < 4; n++) wa[n] = lds0 + ww * 8192 + (g * 8 + q) * 128 + ((n ^ sw) << 5) + p * 8;
  }
  f32x4 acc[4][4];
#pragma unroll
  for (int i = 0; i < 16; i++) {
    acc[i >> 2][i & 3] = f32x4{0.f, 0.f, 0.f, 0.f};
    pin(acc[i >> 2][i & 3]);
  }
  bf16x8 fx[2][4], fw[2][4];
  auto rd = [&](int bf, int kk, u32 so) {
#pragma unroll
    for (int n = 0; n < 4; n++) {
      if (G == 2) fw[bf][n] = lds_b128(wa[kk * 2 + (n >> 1)] + so + (n & 1) * 2048);
      else fw[bf][n] = lds_tr2(wa[n] + so + kk * 4096, wa[n] + so + kk * 4096 + 512);
    }
#pragma unroll
    for (int m = 0; m < 4; m++) fx[bf][m] = lds_b128(xa + so + m * 2048 + kk * 1024);
  };
  auto mm = [&](int bf) {
#pragma unroll
    for (int m = 0; m < 4; m++)
#pragma unroll
      for (int i = 0; i < 4; i++) {
        const int n = (m & 1) ? 3 - i : i;
        acc[m][n] = mfma16(fw[bf][n], fx[bf][m], acc[m][n]);
      }
    sched_rm<G == 2 ? 0 : 4, 16>();
  };
  auto pins = [&]() { pin(xa); pin(wa[0]); pin(wa[1]); pin(wa[2]); pin(wa[3]); };
  auto flip = [&](u32 mx, u32 mw) { xa ^= mx; wa[0] ^= mw; wa[1] ^= mw; wa[2] ^= mw; wa[3] ^= mw; };
  pins();
  asm volatile("" ::"s"(out));
  STAMP(1);
  LBAR(); TL();
  STAMP(2);
  rd(0, 0, 0);
  SB();
  static_assert(S % 2 == 0 && OWN % 2 == 0, "");
  for (int k = S / 2 - 1; k >= 0; k--) {
    WAIT_L(); SB();
    rd(1, 1, 0);
    mm(0);
    LBAR(); TL(); SB();
    rd(0, 0, STAGE);
    mm(1);
    WAIT_L(); SB();
    rd(1, 1, STAGE);
    const u32 t = k ? 0 : ww;
    flip(2 * STAGE | t * 1024, 2 * STAGE | t * (G == 2 ? 8192 + 64 : 8192 + 4096));
    mm(0);
    LBAR(); TL(); SB();
    rd(0, 0, 0);
    mm(1);
  }
  if (ww) {
#pragma unroll
    for (int m = 0; m < 4; m++)
#pragma unroll
      for (int h = 0; h < 2; h++) {
        const f32x4 v = acc[m][2 * h], u = acc[m][2 * h + 1];
        st16(rw, WS_XB + (G == 2 ? 0 : 4u << 20) + pb * 16384 + wx * 8192 + ((h ^ 1) * 4 + m) * 1024 + lane * 16,
             u32x4{pack2(v[0], v[1]), pack2(v[2], v[3]), pack2(u[0], u[1]), pack2(u[2], u[3])});
        acc[m][2 * h] = acc[m][2 * h + 1] = f32x4{0.f, 0.f, 0.f, 0.f};
        pin(acc[m][2 * h]); pin(acc[m][2 * h + 1]);
      }
  }
  const u32 fl = WS_FL + pb * 16 + (G == 2 ? 0 : 8) + wx * 4;
  for (int i = 0; i < OWN / 2 - 1; i++) {
    LBAR(); TL(); SB();
    rd(1, 0, STAGE);
    flip(2 * STAGE, 2 * STAGE);
    mm(0);
    if ((XA & 1) == 0 && i == XA / 2 && ww) {
      WAIT_V(0);
      if (lane == 0) __builtin_amdgcn_raw_buffer_store_b32(1u, rw, fl, 0, 16);
    }
    LBAR(); TL(); SB();
    rd(0, 0, 0);
    mm(1);
    if ((XA & 1) == 1 && i == XA / 2 && ww) {
      WAIT_V(0);
      if (lane == 0) __builtin_amdgcn_raw_buffer_store_b32(1u, rw, fl, 0, 16);
    }
  }
  LBAR(); TL(); SB();
  rd(1, 0, STAGE);
  mm(0);
  const u32 ex0 = (wx ? EX1 : EX0) + lane * 16;
  WAIT_L(); SB();
#pragma unroll
  for (int i = 0; i < 16; i++) {
    const int n = 3 - (i >> 2), m = (n & 1) ? 3 - (i & 3) : (i & 3);
    acc[m][n] = mfma16(fw[1][n], fx[1][m], acc[m][n]);
    if (i >= 4 && i < 12) {
      const int k = i - 4, n = 3 - (k >> 2), m = (n & 1) ? 3 - (k & 3) : (k & 3);
      *(f32x4*)(smem + ex0 + ww * 8192 + (m * 2 + n - 2) * 1024) = acc[m][n];
    }
    SB();
  }
  LBAR(); TL();
  STAMP(3);
  u32x4 res[4], pt[4];
  f32x4 ex[4][2];
#pragma unroll
  for (int m = 0; m < 4; m++) {
    pt[m] = *(const u32x4*)(smem + PT + w * 4096 + m * 1024 + lane * 16);
#pragma unroll
    for (int j = 0; j < 2; j++) ex[m][j] = *(const f32x4*)(smem + ex0 + (ww ^ 1) * 8192 + (m * 2 + j) * 1024);
    const u32 o = G == 2 ? (m * 16 + cc) * 128 + (((ww * 4 + g) ^ (cc & 7)) << 4)
                         : m * 2048 + (ww * 4 + (g & 1) * 2 + (g >> 1)) * 256 + cc * 16;
    res[m] = *(const u32x4*)(smem + (wx ? RES1 : RES0) + o);
  }
  const int e0 = G == 2 ? ww * 32 + g * 8 : ww * 32 + (g & 1) * 16 + (g >> 1) * 8, go = G == 2 ? 0 : 192;
  const f32x4 g0 = *(const f32x4*)(aux + go + e0), g1 = *(const f32x4*)(aux + go + e0 + 4);
  SB();
#pragma unroll
  for (int m = 0; m < 4; m++)
#pragma unroll
    for (int j = 0; j < 2; j++)
      acc[m][j] += f32x4{bf_lo(pt[m][2 * j]), bf_hi(pt[m][2 * j]), bf_lo(pt[m][2 * j + 1]), bf_hi(pt[m][2 * j + 1])} + ex[m][j];

  if (G == 2) {
    const u32 px = (tx * 16 + tw) * 16384 + wx * 8192 + ww * 1024 + lane * 16;
    u32x4 o[4];
    float f[4][8], mean[4], m2[4];
#pragma unroll
    for (int m = 0; m < 4; m++) {
      const f32x4 a = acc[m][0] * g0, bb = acc[m][1] * g1;
      o[m] = u32x4{pack2(bf_lo(res[m][0]) + a[0], bf_hi(res[m][0]) + a[1]), pack2(bf_lo(res[m][1]) + a[2], bf_hi(res[m][1]) + a[3]),
                   pack2(bf_lo(res[m][2]) + bb[0], bf_hi(res[m][2]) + bb[1]), pack2(bf_lo(res[m][3]) + bb[2], bf_hi(res[m][3]) + bb[3])};
#pragma unroll
      for (int i = 0; i < 4; i++) { f[m][2 * i] = bf_lo(o[m][i]); f[m][2 * i + 1] = bf_hi(o[m][i]); }
      mean[m] = (((f[m][0] + f[m][1]) + (f[m][2] + f[m][3])) + ((f[m][4] + f[m][5]) + (f[m][6] + f[m][7]))) * 0.125f;
      m2[m] = 0.f;
#pragma unroll
      for (int i = 0; i < 8; i++) m2[m] += (f[m][i] - mean[m]) * (f[m][i] - mean[m]);
    }
#pragma unroll
    for (int m = 0; m < 2; m++) {
      auto t = __builtin_amdgcn_permlane32_swap(f2u(mean[m]), f2u(mean[m + 2]), false, false);
      auto u = __builtin_amdgcn_permlane32_swap(f2u(m2[m]), f2u(m2[m + 2]), false, false);
      const float d = u2f(t[0]) - u2f(t[1]);
      mean[m] = 0.5f * (u2f(t[0]) + u2f(t[1]));
      m2[m] = u2f(u[0]) + u2f(u[1]) + d * d * 4.f;
    }
    {
      auto t = __builtin_amdgcn_permlane16_swap(f2u(mean[0]), f2u(mean[1]), false, false);
      auto u = __builtin_amdgcn_permlane16_swap(f2u(m2[0]), f2u(m2[1]), false, false);
      const float d = u2f(t[0]) - u2f(t[1]);
      *(f32x2*)(aux + 2560 + ww * 256 + (wx * 64 + lane) * 2) =
          f32x2{0.5f * (u2f(t[0]) + u2f(t[1])), u2f(u[0]) + u2f(u[1]) + d * d * 8.f};
    }
    LBAR(); TL();
#pragma unroll
    for (int m = 0; m < 4; m++) st16<SPX2>(rw, WS_X2 + px + m * 2048, o[m]);
    const f32x4 s0 = *(const f32x4*)(aux + 64 + e0), s1 = *(const f32x4*)(aux + 64 + e0 + 4);
    const f32x4 c0 = *(const f32x4*)(aux + 128 + e0), c1 = *(const f32x4*)(aux + 128 + e0 + 4);
    STAMP(4);
    LBAR(); TL();
#pragma unroll
    for (int m = 0; m < 4; m++) {
      const f32x2 st = *(const f32x2*)(aux + 3072 + (wx * 64 + m * 16 + cc) * 2);
      float h[8];
#pragma unroll
      for (int i = 0; i < 8; i++) h[i] = (f[m][i] - st[0]) * st[1] * (i < 4 ? c0[i & 3] : c1[i & 3]) + (i < 4 ? s0[i & 3] : s1[i & 3]);
      st16<SPH2>(rw, WS_H2 + px + m * 2048, u32x4{pack2(h[0], h[1]), pack2(h[2], h[3]), pack2(h[4], h[5]), pack2(h[6], h[7])});
    }
  } else {
    const rsrc_t ro = mkrsrc(out, S_ * D_ * 2);
#pragma unroll
    for (int m = 0; m < 4; m++) {
      f32x4 a, bb;
#pragma unroll
      for (int i = 0; i < 4; i++) {
        auto sw = __builtin_amdgcn_permlane16_swap(f2u(acc[m][0][i]), f2u(acc[m][1][i]), false, false);
        a[i] = u2f(sw[0]);
        bb[i] = u2f(sw[1]);
      }
      a *= g0; bb *= g1;
      u32x4 o = {pack2(bf_lo(res[m][0]) + a[0], bf_hi(res[m][0]) + a[1]), pack2(bf_lo(res[m][1]) + a[2], bf_hi(res[m][1]) + a[3]),
                 pack2(bf_lo(res[m][2]) + bb[0], bf_hi(res[m][2]) + bb[1]), pack2(bf_lo(res[m][3]) + bb[2], bf_hi(res[m][3]) + bb[3])};
      st16<SPG4>(ro, (tx * 128 + wx * 64 + m * 16 + cc) * 2048 + (tw * 64 + e0) * 2, o);
    }
  }
  STAMP_OUT(G, w, (G == 2 ? 5 : 4));
  TL_OUT(G, w);
}

constexpr int G3_UP = 14336 + 16, G3_WS = 2 * G3_UP, G3_WOFF = 65536, G3_PF = G3_WOFF + 3 * G3_WS, G3_LDS = G3_PF + 256;
DI float geglu(float gt, float up) {
  const float k1 = -2.302208198f, k3 = -0.1029432397f;
  const float e = __builtin_amdgcn_exp2f(gt * (k1 + k3 * gt * gt));
  return gt * __builtin_amdgcn_rcpf(1.f + e) * up;
}
__global__ __launch_bounds__(512) void gemm_g3(const char* __restrict__ Wg, const char* __restrict__ Wu,
                                               const char* __restrict__ Wd, char* __restrict__ ws) {
  constexpr int N = 16;
  STAMP_DECL
  extern __shared__ __attribute__((aligned(16))) char smem[];
  const u32 lds0 = (u32)(size_t)(LDS_AS char*)smem;
  const int b = blockIdx.x, xcd = b & 7, r = b >> 3, ri = r & 3, rj = r >> 2;
#define MAP3 0
  const int tm = (xcd & 1) * 4 + ri, tn = (xcd >> 1) * 8 + rj, c = (ri + rj) & 3;
  const int tid = threadIdx.x, lane = tid & 63;
  const int w = __builtin_amdgcn_readfirstlane(tid >> 6);
  const int grp = w >> 2, lw = w & 3, wx = lw >> 1, wn = lw & 1, g = lane >> 4, cc = lane & 15;

  TL_DECL
  u32 vo[7];
  const char* gb;
  if (grp == 0) {
    gb = (lw < 2 ? Wg : Wu) + tn * 224;
#pragma unroll
    for (int i = 0; i < 7; i++) {
      const int pi = ((lw & 1) * 7 + i) * 64 + lane, pr = pi / 14, ch = pi - pr * 14;
      const int k = (pr & ~12) | ((pr & 4) << 1) | ((pr & 8) >> 1);
      vo[i] = k * 7168 + ch * 16 - (i & 3) * 1024;
    }
  } else {
    gb = ws + WS_H2 + (size_t)tm * 2 * N * 16384;
#pragma unroll
    for (int i = 0; i < 7; i++) vo[i] = lane * 16 + lw * 4096;
  }
  auto issueW = [&](int q, int slot) {
    const char* p = gb + (size_t)kstep<KM3>(q, c, N) * (64 * 7168);
    const u32 d = lds0 + G3_WOFF + slot * G3_WS + (lw >> 1) * G3_UP + (lw & 1) * 7168;
    glds4(p, d, vo[0], vo[1], vo[2], vo[3]);
    glds3(p, d + 4096, vo[4], vo[5], vo[6]);
  };
  auto issueX = [&](int q, int half) {
    const char* p = gb + (size_t)(half * N + kstep<KM3>(q, c, N)) * 16384;
    glds4(p, lds0 + half * 32768 + (q & 1) * 16384 + lw * 4096, vo[0], vo[0], vo[0], vo[0]);
  };
  const u32 xa = lds0 + grp * 32768 + wx * 8192 + lane * 16;
  const int q4 = cc >> 2, p4 = lane & 3;
  const u32 rowb = lds0 + G3_WOFF + ((g >> 1) * 16 + (g & 1) * 4 + q4) * 224 + (p4 & 1) * 8;
  const u32 wa = rowb + (p4 >> 1) * 16 + wn * 96;
  const u32 wm = rowb + (p4 >> 1) * G3_UP + (12 + wn) * 16;
  f32x4 acc[4][7];
#pragma unroll
  for (int m = 0; m < 4; m++)
#pragma unroll
    for (int n = 0; n < 7; n++) acc[m][n] = f32x4{0.f, 0.f, 0.f, 0.f};
  bf16x8 fx[2][4], fw[2][7];
  auto rd = [&](int k, int slot, auto np, const char* p0, u32 d0, const char* p1, u32 d1) {
    constexpr int NP = decltype(np)::value;
    const u32 xo = xa + (k & 1) * 16384, wo = wa + slot * G3_WS, mo = wm + slot * G3_WS;
    auto piece = [&](auto ic) {
      constexpr int I = decltype(ic)::value;
      if constexpr (I < NP) glds1<(I & 3) * 1024>(I < 4 ? p0 : p1, I < 4 ? d0 : d1, vo[I < 7 ? I : 0]);
    };
    auto rw = [&](int kk, int n) {
      const u32 a = wo + kk * 7168 + (n / 3) * G3_UP + (n % 3) * 32;
      fw[kk][n] = lds_tr2(a, a + 1792);
    };
    auto half = [&](auto kc) {
      constexpr int kk = decltype(kc)::value;
      piece(IC<kk * 4>{});
      fx[kk][0] = lds_b128(xo + kk * 1024); rw(kk, 0); rw(kk, 1);
      piece(IC<kk * 4 + 1>{});
      fx[kk][1] = lds_b128(xo + 2048 + kk * 1024); rw(kk, 2); rw(kk, 3);
      piece(IC<kk * 4 + 2>{});
      fx[kk][2] = lds_b128(xo + 4096 + kk * 1024); rw(kk, 4); rw(kk, 5);
      piece(IC<kk * 4 + 3>{});
      fx[kk][3] = lds_b128(xo + 6144 + kk * 1024);
      fw[kk][6] = lds_tr2(mo + kk * 7168, mo + kk * 7168 + 1792);
    };
    half(IC<0>{});
    half(IC<1>{});
  };
#define EB 1
  auto mm = [&](auto wv) {
    constexpr int WV = decltype(wv)::value;
    __builtin_amdgcn_s_setprio(1);
#pragma unroll
    for (int i = 0; i < 56; i++) {
      const int kk = i / 28, n = (i % 28) / 4, m = (n & 1) ? 3 - (i & 3) : (i & 3);
      if (i == 56 - EB && WV >= 0) {
        SB();
        WAIT_V(WV);
        BAR();
        if (EB > 1) __builtin_amdgcn_s_setprio(3);
        SB();
      }
      acc[m][n] = mfma16(fw[kk][n], fx[kk][m], acc[m][n]);
    }
    __builtin_amdgcn_s_setprio(0);
    SB();
  };
  if (grp == 0) {
    auto wsrc = [&](int q) { return gb + (size_t)kstep<KM3>(q, c, N) * (64 * 7168); };
    const u32 wd = lds0 + G3_WOFF + (lw >> 1) * G3_UP + (lw & 1) * 7168;
    issueW(0, 0);
    STAMP(1);
    WAIT_V(0);
    BAR();
    STAMP(2);
    issueW(1, 1);
    int slot = 0;
    for (int k = 0; k < N - 2; k++) {
      const int s2 = slot == 0 ? 2 : slot - 1;
      const char* p = wsrc(k + 2);
      rd(k, slot, IC<7>{}, p, wd + s2 * G3_WS, p, wd + s2 * G3_WS + 4096);
      if (k == 2 + 3 * lw && lane < 56) glds_dw(Wd, lds0 + G3_PF, ((b * 4 + lw) * 56 + lane) * 128);
      SB(); LBAR(); TL(); SB();
      mm(IC<7>{});
      slot = slot == 2 ? 0 : slot + 1;
    }
    rd(N - 2, (N - 2) % 3, IC<0>{}, gb, 0, gb, 0);
    SB(); LBAR(); TL(); SB();
    mm(IC<0>{});
    rd(N - 1, (N - 1) % 3, IC<0>{}, gb, 0, gb, 0);
    SB(); LBAR(); TL(); SB();
    mm(IC<0>{});
  } else {
    auto xsrc = [&](int q, int half) { return gb + (size_t)(half * N + kstep<KM3>(q, c, N)) * 16384; };
    const u32 xd = lds0 + lw * 4096;
    issueX(0, 0);
    issueX(0, 1);
    STAMP(1);
    WAIT_V(4);
    BAR();
    issueX(1, 0);
    WAIT_V(4);
    BAR();
    STAMP(2);
    int slot = 0;
    for (int k = 0; k < N - 2; k++) {
      rd(k, slot, IC<8>{}, xsrc(k + 1, 1), xd + 32768 + ((k + 1) & 1) * 16384, xsrc(k + 2, 0), xd + (k & 1) * 16384);
      SB();
      WAIT_V(8);
      LBAR(); TL(); SB();
      mm(IC<4>{});
      slot = slot == 2 ? 0 : slot + 1;
    }
    rd(N - 2, (N - 2) % 3, IC<4>{}, xsrc(N - 1, 1), xd + 32768 + ((N - 1) & 1) * 16384, gb, 0);
    SB();
    WAIT_V(4);
    LBAR(); TL(); SB();
    mm(IC<0>{});
    rd(N - 1, (N - 1) % 3, IC<0>{}, gb, 0, gb, 0);
    SB(); LBAR(); TL(); SB();
    mm(IC<-1>{});
  }
  STAMP(3);
  const rsrc_t rw = mkrsrc(ws);
  const u32 pb = WS_P + (tm * 2 + grp) * 56 * 16384 + wx * 8192 + cc * 16;
  auto st = [&](int mt, int chunk, u32x4 v) {
    const int Gs = tn * 14 + chunk;
    st16<SPG3>(rw, pb + (Gs >> 3) * 16384 + mt * 2048 + (Gs & 7) * 256, v);
  };
  u32 t2[4][2];
#pragma unroll
  for (int m = 0; m < 4; m++) {
    u32 rr[3][2];
#pragma unroll
    for (int t = 0; t < 3; t++)
#pragma unroll
      for (int j = 0; j < 2; j++)
        rr[t][j] = pack2(geglu(acc[m][t][2 * j], acc[m][3 + t][2 * j]), geglu(acc[m][t][2 * j + 1], acc[m][3 + t][2 * j + 1]));
    auto s0 = __builtin_amdgcn_permlane16_swap(rr[0][0], rr[1][0], false, false);
    auto s1 = __builtin_amdgcn_permlane16_swap(rr[0][1], rr[1][1], false, false);
    st(m, wn * 6 + (g & 1) * 2 + (g >> 1), u32x4{s0[0], s1[0], s0[1], s1[1]});
    t2[m][0] = rr[2][0];
    t2[m][1] = rr[2][1];
  }
  u32 mx[2][2];
#pragma unroll
  for (int h = 0; h < 2; h++) {
    auto s0 = __builtin_amdgcn_permlane16_swap(t2[2 * h][0], t2[2 * h + 1][0], false, false);
    auto s1 = __builtin_amdgcn_permlane16_swap(t2[2 * h][1], t2[2 * h + 1][1], false, false);
    st(2 * h + (g & 1), wn * 6 + 4 + (g >> 1), u32x4{s0[0], s1[0], s0[1], s1[1]});
    float o[4];
#pragma unroll
    for (int i = 0; i < 4; i++) {
      auto sw = __builtin_amdgcn_permlane32_swap(f2u(acc[2 * h][6][i]), f2u(acc[2 * h + 1][6][i]), false, false);
      o[i] = geglu(u2f(sw[0]), u2f(sw[1]));
    }
    mx[h][0] = pack2(o[0], o[1]);
    mx[h][1] = pack2(o[2], o[3]);
  }
  {
    auto s0 = __builtin_amdgcn_permlane16_swap(mx[0][0], mx[1][0], false, false);
    auto s1 = __builtin_amdgcn_permlane16_swap(mx[0][1], mx[1][1], false, false);
    st((g & 1) * 2 + (g >> 1), 12 + wn, u32x4{s0[0], s1[0], s0[1], s1[1]});
  }
  STAMP_OUT(3, w, 4);
  TL_OUT(3, w);
}

extern "C" void kernel_launch(void* const* d_in, const int* in_sizes, int n_in, void* d_out, int out_size, void* d_ws,
                              size_t ws_size, hipStream_t stream) {
  const char* x = (const char*)d_in[0];
  char* ws = (char*)d_ws;
  (void)hipFuncSetAttribute((const void*)gemm_g1, hipFuncAttributeMaxDynamicSharedMemorySize, PC_LDS);
  (void)hipFuncSetAttribute((const void*)gemm_sk<2>, hipFuncAttributeMaxDynamicSharedMemorySize, PC_LDS);
  (void)hipFuncSetAttribute((const void*)gemm_sk<4>, hipFuncAttributeMaxDynamicSharedMemorySize, PC_LDS);
  (void)hipFuncSetAttribute((const void*)gemm_g3, hipFuncAttributeMaxDynamicSharedMemorySize, G3_LDS);
  k0_prep<<<256, 512, 0, stream>>>((const u16*)x, (const u16*)d_in[9], (const u16*)d_in[10], ws);
  gemm_g1<<<256, 512, PC_LDS, stream>>>((const char*)d_in[1], (const char*)d_in[2], ws, (const char*)d_in[3],
                                        (const char*)d_in[4], (const char*)d_in[6], (const char*)d_in[7]);
  gemm_sk<2><<<256, 512, PC_LDS, stream>>>((const char*)d_in[3], (const char*)d_in[4], x, ws, (char*)d_out,
                                           (const char*)d_in[6], (const char*)d_in[7]);
  gemm_g3<<<256, 512, G3_LDS, stream>>>((const char*)d_in[6], (const char*)d_in[7], (const char*)d_in[8], ws);
  gemm_sk<4><<<256, 512, PC_LDS, stream>>>((const char*)d_in[8], (const char*)d_in[8], x, ws, (char*)d_out, x, x);
}
```

```cpp
#include <hip/hip_runtime.h>
#include <hip/hip_bf16.h>
#pragma clang diagnostic ignored "-Winline-asm"

typedef unsigned short u16;
typedef unsigned int u32;
typedef unsigned long u64;
typedef __bf16 bf16x8 __attribute__((ext_vector_type(8)));
typedef __bf16 bf16x2 __attribute__((ext_vector_type(2)));
typedef short s16x4 __attribute__((ext_vector_type(4)));
typedef float f32x4 __attribute__((ext_vector_type(4)));
typedef float f32x2 __attribute__((ext_vector_type(2)));
typedef u32 u32x4 __attribute__((ext_vector_type(4)));
typedef u32 u32x2 __attribute__((ext_vector_type(2)));
#define LDS_AS __attribute__((address_space(3)))
#define DI __device__ __forceinline__

constexpr int S_ = 2048, D_ = 1024, F_ = 3584;
constexpr int KS = 8;

constexpr size_t WS_MODP = 0;
constexpr size_t WS_ST = 262144;
constexpr size_t WS_NT = 1u << 20;
constexpr size_t WS_PP = WS_NT + (4u << 20);
constexpr size_t WS_H2 = WS_PP + (8u << 20);
constexpr size_t WS_P = WS_H2 + (4u << 20);
constexpr size_t WS_XB = WS_P + (14u << 20);
#define FLS 128
constexpr size_t WS_FL = WS_XB + (8u << 20);
constexpr u32 FLK = 256 * FLS;
#define REP 8
constexpr size_t WS_EP = WS_FL + 4 * FLK;
constexpr size_t WS_F0 = WS_EP + 2048;

DI float bf_lo(u32 v) { return __builtin_bit_cast(float, v << 16); }
DI float bf_hi(u32 v) { return __builtin_bit_cast(float, v & 0xffff0000u); }
DI u32 pack2(float a, float b) {
  f32x2 v = {a, b};
  return __builtin_bit_cast(u32, __builtin_convertvector(v, bf16x2));
}
DI u32 f2u(float a) { return __builtin_bit_cast(u32, a); }
DI float u2f(u32 a) { return __builtin_bit_cast(float, a); }
DI float rbf(float a) { return bf_lo(pack2(a, a)); }
DI float sum32(float v) {
  auto t = __builtin_amdgcn_permlane32_swap(f2u(v), f2u(v), false, false);
  return u2f(t[0]) + u2f(t[1]);
}
template <int C>
DI float dpp(float v) { return u2f(__builtin_amdgcn_update_dpp(0u, f2u(v), C, 0xf, 0xf, true)); }
DI float wave_sum(float v) {
  v += dpp<0xb1>(v); v += dpp<0x4e>(v); v += dpp<0x141>(v); v += dpp<0x140>(v);
  auto t = __builtin_amdgcn_permlane16_swap(f2u(v), f2u(v), false, false);
  return sum32(u2f(t[0]) + u2f(t[1]));
}
DI f32x4 mfma16(bf16x8 a, bf16x8 b, f32x4 c) { return __builtin_amdgcn_mfma_f32_16x16x32_bf16(a, b, c, 0, 0, 0); }
DI bf16x8 lds_b128(u32 addr) { return *(const LDS_AS bf16x8*)(size_t)addr; }
DI bf16x8 lds_tr2(u32 a0, u32 a1) {
  s16x4 lo = __builtin_amdgcn_ds_read_tr16_b64_v4i16((LDS_AS s16x4*)(size_t)a0);
  s16x4 hi = __builtin_amdgcn_ds_read_tr16_b64_v4i16((LDS_AS s16x4*)(size_t)a1);
  u32x2 l = __builtin_bit_cast(u32x2, lo), h = __builtin_bit_cast(u32x2, hi);
  u32x4 r = {l[0], l[1], h[0], h[1]};
  return __builtin_bit_cast(bf16x8, r);
}
#define WAIT_V(n) asm volatile("s_waitcnt vmcnt(%0)" ::"i"(n) : "memory")
#define BAR() asm volatile("s_barrier" ::: "memory")
#define WAIT_L() __builtin_amdgcn_s_waitcnt(0xC07F)
#define LBAR() do { asm volatile("" ::: "memory"); WAIT_L(); BAR(); } while (0)

struct Dg { u32 c, r, t; };
#define STAMP(i)
#define STAMP_OUT(kid, i)
#define TLI "s_memtime"
#define NSG 106
#define TL(ph)

DI void glds4(const char* sbase, u32 m0v, u32 v0, u32 v1, u32 v2, u32 v3) {
  asm volatile(
      "s_mov_b32 m0, %1\n\ts_nop 0\n\t"
      "global_load_lds_dwordx4 %2, %0\n\t"
      "global_load_lds_dwordx4 %3, %0 offset:1024\n\t"
      "global_load_lds_dwordx4 %4, %0 offset:2048\n\t"
      "global_load_lds_dwordx4 %5, %0 offset:3072" ::"s"(sbase),
      "s"(m0v), "v"(v0), "v"(v1), "v"(v2), "v"(v3)
      : "memory");
}
DI void glds3(const char* sbase, u32 m0v, u32 v0, u32 v1, u32 v2) {
  asm volatile(
      "s_mov_b32 m0, %1\n\ts_nop 0\n\t"
      "global_load_lds_dwordx4 %2, %0\n\t"
      "global_load_lds_dwordx4 %3, %0 offset:1024\n\t"
      "global_load_lds_dwordx4 %4, %0 offset:2048" ::"s"(sbase),
      "s"(m0v), "v"(v0), "v"(v1), "v"(v2)
      : "memory");
}
DI void glds2(const char* sbase, u32 m0v, u32 v0, u32 v1) {
  asm volatile(
      "s_mov_b32 m0, %1\n\ts_nop 0\n\t"
      "global_load_lds_dwordx4 %2, %0\n\t"
      "global_load_lds_dwordx4 %3, %0 offset:1024" ::"s"(sbase),
      "s"(m0v), "v"(v0), "v"(v1)
      : "memory");
}
template <int OFF, int DEV>
DI void glds1(const char* sbase, u32 m0v, u32 v0) {
  if (DEV) asm volatile("s_mov_b32 m0, %1\n\ts_nop 0\n\tglobal_load_lds_dwordx4 %2, %0 offset:%3 sc1" ::"s"(sbase), "s"(m0v), "v"(v0), "i"(OFF) : "memory");
  else asm volatile("s_mov_b32 m0, %1\n\ts_nop 0\n\tglobal_load_lds_dwordx4 %2, %0 offset:%3" ::"s"(sbase), "s"(m0v), "v"(v0), "i"(OFF) : "memory");
}
template <int OFF, int DEV>
DI void dma(const char* sbase, u32 v0) {
  if (DEV) asm volatile("global_load_lds_dwordx4 %1, %0 offset:%2 sc1" ::"s"(sbase), "v"(v0), "i"(OFF) : "memory");
  else asm volatile("global_load_lds_dwordx4 %1, %0 offset:%2" ::"s"(sbase), "v"(v0), "i"(OFF) : "memory");
  __builtin_amdgcn_sched_barrier(0);
}
template <int OFF>
DI void setm0(u32 base) { asm volatile("s_add_u32 m0, %0, %1\n\ts_nop 0" ::"s"(base), "i"(OFF) : "scc", "memory"); }
DI void pins(const char*& p) { asm volatile("" : "+s"(p)); }
DI void glds4_dev(const char* sbase, u32 m0v, u32 v0, u32 v1, u32 v2, u32 v3) {
  asm volatile(
      "s_mov_b32 m0, %1\n\ts_nop 0\n\t"
      "global_load_lds_dwordx4 %2, %0 sc1\n\t"
      "global_load_lds_dwordx4 %3, %0 offset:1024 sc1\n\t"
      "global_load_lds_dwordx4 %4, %0 offset:2048 sc1\n\t"
      "global_load_lds_dwordx4 %5, %0 offset:3072 sc1" ::"s"(sbase),
      "s"(m0v), "v"(v0), "v"(v1), "v"(v2), "v"(v3)
      : "memory");
}
DI void glds4_dev(const char* sbase, u32 m0v, u32 v0) { glds4_dev(sbase, m0v, v0, v0, v0, v0); }
DI void glds_dw(const char* sbase, u32 m0v, u32 v0) {
  asm volatile("s_mov_b32 m0, %1\n\ts_nop 0\n\tglobal_load_lds_dword %2, %0" ::"s"(sbase), "s"(m0v), "v"(v0) : "memory");
}

typedef __amdgpu_buffer_rsrc_t rsrc_t;
DI rsrc_t mkrsrc(const void* p, int bytes = 0x4000000) { return __builtin_amdgcn_make_buffer_rsrc((void*)p, 0, bytes, 0x00020000); }
template <int AUXV = 16>
DI void st16(rsrc_t r, u32 off, u32x4 v) { __builtin_amdgcn_raw_buffer_store_b128(v, r, off, 0, AUXV); }
DI void stouch2(const char* p) {
  asm volatile("s_load_dwordx2 s[100:101], %0, 60\n\ts_load_dwordx2 s[100:101], %0, 188" ::"s"(p) : "s100", "s101");
}
DI int kstep0(int q, int c, int n) {
  if (q == 0) return c;
  int t = q - 1;
  if (t + 4 >= n + ((t - c) & 3) && t >= c) return q;
  return ((t & 3) == c) ? t + 4 : t;
}
template <int KM>
DI int kstep(int q, int c, int n) {
  if (KM == 0) return kstep0(q, c, n);
  if (KM == 9) return q;
  if (KM >= 10) {
    constexpr int L = KM % 10, F = KM / 10;
    const int d = (q + L - c) & 3, s = q + L - d;
    if (s < F || s >= n || d > L) return q;
    return d == 0 ? s : q - 1;
  }
  return q < KM ? q : KM + kstep0(q - KM, c, n - KM);
}
#define KMP 1
#define KMS 42
#define KMO 22
#define KMS4 43
#define KMO4 KMO
#define KM3 31

constexpr u32 EMPTY = 0xffffffffu;
constexpr int SPIN_CAP = 1 << 16;
constexpr int STAGE = 32768, WO = 16384;
constexpr int G3_WA = 4 * STAGE, G3_WB = 2 * STAGE + WO;
constexpr int AUX = 159744, LDS_BYTES = 163840;
template <int V> struct IC { static constexpr int value = V; };
template <int A, int B, class F>
DI void sfor(F f) {
  if constexpr (A < B) { f(IC<A>{}); sfor<A + 1, B>(f); }
}
#define SB() __builtin_amdgcn_sched_barrier(0)
template <int N2, int NM>
DI void sched_rm() {
#pragma unroll
  for (int i = 0; i < NM; i++) {
    __builtin_amdgcn_sched_group_barrier(0x008, 1, 0);
    if (i < N2) __builtin_amdgcn_sched_group_barrier(0x100, 2, 0);
    else if (i < 8) __builtin_amdgcn_sched_group_barrier(0x100, 1, 0);
    else __builtin_amdgcn_sched_group_barrier(0x006, 2, 0);
  }
  SB();
}
DI void pin(u32& v) { asm volatile("" : "+v"(v)); }
DI void pin(f32x2& v) { asm volatile("" : "+v"(v)); }
DI void pin(f32x4& v) { asm volatile("" : "+v"(v)); }
DI void pin(u32x4& v) { asm volatile("" : "+v"(v)); }
DI int lane_id() {
  u32 l;
  asm volatile("v_mbcnt_lo_u32_b32 %0, -1, 0\n\tv_mbcnt_hi_u32_b32 %0, -1, %0" : "=v"(l));
  return l;
}
struct Ctx {
  const char *x, *wsr, *wsi, *wdr, *wdi, *wg, *wu, *wd, *cv, *wm;
  char *ws, *out, *smem;
  int b, ri, rj, xcd, w;
};
DI void lds_inc(int i) {
  asm volatile("s_mov_b64 exec, 1\n\tds_add_u32 %0, %1\n\ts_mov_b64 exec, -1" ::"v"(AUX + i * 4), "v"(1) : "memory");
}
template <int NV = 0>
DI bool last_of_4(int kind) {
  u32 a = AUX + (768 + kind) * 4, v = 1, old;
  pin(a); pin(v);
  WAIT_V(NV);
  asm volatile("s_mov_b64 exec, 1\n\tds_add_rtn_u32 %1, %2, %1\n\ts_mov_b64 exec, -1\n\ts_waitcnt lgkmcnt(0)\n\tv_readfirstlane_b32 %0, %1"
               : "=s"(old), "+v"(v) : "v"(a) : "memory");
  return old == 3;
}
template <int NV = 0>
DI void publish(const Ctx& c, int kind, int lane, int n, u32 off) {
  if (lane >= n) off = 0x7ffffff0u;
  pin(off);
  if (last_of_4<NV>(kind)) __builtin_amdgcn_raw_buffer_store_b32(1u, mkrsrc(c.ws), off, 0, 16);
}
DI u32 peek(const Ctx& c, u32 off) {
  u32 f;
  asm volatile("global_load_dword %0, %1, %2 sc1" : "=v"(f) : "v"(off), "s"(c.ws) : "memory");
  return f;
}
#define PS0 ""
#define PS1 ""
#define POLL(cmp, ld, t)                                                                                              \
  u32 n;                                                                                                              \
  asm volatile("s_mov_b32 %1, %5\n1:\n\t" cmp " vcc, %4, %0\n\ts_and_b64 vcc, vcc, exec\n\ts_cbranch_scc0 2f\n\t" ld   \
               " %0, %2, %3 sc1\n\ts_sub_u32 %1, %1, 1\n\ts_waitcnt vmcnt(0)\n\ts_cbranch_scc0 1b\n2:"               \
               : "+&v"(f), "=&s"(n) : "v"(off), "s"(c.ws), "s"(t), "i"(SPIN_CAP) : "vcc", "scc", "memory")
DI void await(const Ctx& c, u32 off, u32& f) { POLL("v_cmp_eq_u32", PS1 "global_load_dword", EMPTY); }
DI void await(const Ctx& c, u32 off, u64& f, u64 tag) { POLL("v_cmp_ne_u64", PS0 "global_load_dwordx2", tag); }
#define PD 0
#define SL9 0
#define ONEP 1
DI void await1(const Ctx& c, u32 off) {
  u32 f = EMPTY;
  if (!ONEP || c.w == 4) {
    if (PD) {
      u32 a = AUX + 769 * 4, v;
      asm volatile("1:\n\tds_read_b32 %0, %1\n\ts_waitcnt lgkmcnt(0)\n\tv_cmp_gt_u32 vcc, %2, %0\n\ts_and_b64 vcc, vcc, exec\n\ts_cbranch_scc1 1b"
                   : "=&v"(v) : "v"(a), "i"(PD) : "vcc", "scc", "memory");
      if (SL9) __builtin_amdgcn_s_sleep(SL9);
    }
    await(c, off, f);
  }
  if (ONEP) BAR();
}

DI void release(int i, int lw) {
  u32 go = AUX + i * 4, tv;
  asm volatile("s_cmp_eq_u32 %2, 0\n\ts_cbranch_scc1 2f\n1:\n\tds_read_b32 %0, %1\n\ts_waitcnt lgkmcnt(0)\n\t"
               "v_cmp_eq_u32 vcc, 0, %0\n\ts_and_b64 vcc, vcc, exec\n\ts_cbranch_scc1 1b\n2:\n\tds_write_b32 %1, %1"
               : "=&v"(tv) : "v"(go), "s"(lw) : "vcc", "scc", "memory");
}

#define FE 1
#define PE 0
#define OW4 12
#define XA 2
#define XB 6
#define NB 3
#define PRB 2
#define JP 7
#define PRA 2
#define NE 8
#define NS 1
#define NPK 3
#define CPF 1
#define H1E 0
#define EP 0
#define XE3 0
#define W3A 0
#define SL7 12
#define XB3 1
#define W3P 1
#define SP 1
#define X2E 1
#define W3E 1
#define PB 2
constexpr int JL = (8 - NE + NS - 1) / NS - 1, JF = (JL + PB) | 1;
static_assert(PB >= 2 && JF < 8, "");
template <int G>
struct Sk {
  static constexpr int N = G == 2 ? 32 : 56, OWN = G == 2 ? 12 : OW4, S = (N - OWN) / 2, NQ = S + OWN;
  static constexpr int PT = PE ? G3_WA : (NQ & 3) * STAGE, EX1 = ((NQ + 1) & 3) * STAGE, EX0 = ((NQ + 2) & 3) * STAGE;
  static constexpr u32 XBUF = WS_XB + (G == 2 ? 0 : 4u << 20);
  const Ctx& c;
  int tx, tw, lw;
  u32 vw[4], vx, tab, vm, ln;
  const char* X;
  DI Sk(const Ctx& c_) : c(c_) {}
  DI void init(int lane) {
    tx = (c.xcd & 3) * 4 + c.ri; tw = c.rj * 2 + (c.xcd >> 2); lw = c.w & 3;
    const int rr = lane >> 3;
    vx = lane * 16 + lw * 4096;
    X = c.ws + (G == 2 ? WS_PP : WS_P) + (size_t)tx * (N * 16384);
#pragma unroll
    for (int i = 0; i < 4; i++) {
      const int t = tw ^ (i >> 1), j = i & 1;
      if (G == 2) {
        const int row = t * 64 + (lw >> 1) * 32 + (lw & 1) * 4 + j * 8 + (rr >> 2) * 16 + (rr & 3);
        vw[i] = row * 2048 + (((lane & 7) ^ rr) << 4) - j * 1024;
      } else {
        const int row = (lw * 2 + j) * 8 + rr;
        const int sw = (((row >> 1) & 1) | (((row >> 3) & 1) << 1)) << 1;
        vw[i] = row * 2048 + t * 128 + (((lane & 7) ^ sw) << 4) - j * 1024;
      }
    }
    const int cl = (c.ri + c.rj) & 3, q = lane, SA = G == 2 ? 0 : 12;
    int s = q < S ? kstep<G == 2 ? KMS : KMS4>(q - SA, cl, S - SA - (G == 2)) : S - SA + kstep<G == 2 ? KMO : KMO4>(q - S, cl, OWN);
    const int h = s < 16 ? tw & 1 : ~tw & 1;
    if (s >= 16) s -= OWN / 2;
    if (G == 2) {
      const int o = s >= 9, p = s - 2 - 7 * o;
      s = s < 2 ? s + 2 * c.rj : 2 * (p + (p >= c.rj)) + o;
    }
    tab = q < SA ? 32 + (tw & 1) * 12 + kstep<KMS4>(q, cl, SA) : s + h * 16;
    if (G == 2) {
      vm = (lane >> 4) * 24576 + (lane & 15) * 16 + ((lw >> 1) * 1024 + c.rj * 128 + (lw & 1) * 64) * 4;
      ln = lane;
      pin(vm); pin(ln);
    }
    pin(tab); pin(vx); pin(vw[0]); pin(vw[1]); pin(vw[2]); pin(vw[3]);
  }
  DI u32 blk(int q) const { return __builtin_amdgcn_readlane(tab, q); }
  DI const char* wsrc(u32 s) const { return G == 2 ? (s < 16 ? c.wdr : c.wdi) + (s & 15) * 128 : c.wd + (size_t)s * (64 * 2048); }
  DI u32 wdst(int q) const { return (q & 3) * STAGE + WO + lw * 2048; }
  template <bool SH, int WX = 3>
  DI void issue(int q) const {
    const u32 s = blk(q);
    if (WX & 1) {
      glds2(wsrc(s), wdst(q), vw[0], vw[1]);
      if (SH) glds2(wsrc(s), wdst(q) + 8192, vw[2], vw[3]);
    }
    if (WX & 2) glds4_dev(X + (size_t)s * 16384, (q & 3) * STAGE + lw * 4096, vx);
  }
};
struct T3 {
  int tm, tn, cl, lw;
  DI T3(const Ctx& c) { tm = (c.xcd & 1) * 4 + c.ri; tn = (c.xcd >> 1) * 8 + c.rj; cl = (c.ri + c.rj) & 3; lw = c.w & 3; }
  DI u32 aoff(int i, int lane) const {
    const int pi = ((lw & 1) * 3 + i) * 64 + lane, pr = (pi * 10923) >> 16, ch = pi - pr * 6;
    return ((pr & ~12) | ((pr & 4) << 1) | ((pr & 8) >> 1)) * 7168 + ch * 16 - i * 1024;
  }
  DI u32 boff(int i, int lane) const {
    const int rr = lane >> 3;
    return ((lw * 2 + i) * 8 + rr) * 7168 + (((lane & 7) ^ ((rr & 2) | i * 4)) << 4) - i * 1024;
  }
  DI u32 ks(int q) const { return kstep<KM3>(q, cl, 16); }
  DI const char* asrc(const Ctx& c, u32 s) const { return (lw < 2 ? c.wg : c.wu) + 4096 + tn * 96 + (size_t)s * (64 * 7168); }
  DI const char* bsrc(const char* w, u32 s) const { return w + tn * 128 + (size_t)s * (64 * 7168); }
  DI u32 adst(int q) const { return G3_WA + (q & 1) * 12288 + lw * 3072; }
  DI u32 bdst(int q) const { return G3_WB + (q & 1) * STAGE + lw * 2048; }
};

constexpr int SM = G3_WA, SMS = 2112;
DI void ln_row(const Ctx& c, int lane) {
  u32x4* row = (u32x4*)(c.smem + SM + c.w * SMS) + lane;
  const u32x4 v0 = row[0], v1 = row[64];
  float f[16];
#pragma unroll
  for (int i = 0; i < 4; i++) {
    f[2 * i] = bf_lo(v0[i]); f[2 * i + 1] = bf_hi(v0[i]);
    f[8 + 2 * i] = bf_lo(v1[i]); f[8 + 2 * i + 1] = bf_hi(v1[i]);
  }
  float s[4], q[4];
#pragma unroll
  for (int i = 0; i < 4; i++) s[i] = (f[i] + f[4 + i]) + (f[8 + i] + f[12 + i]);
  const float m = wave_sum((s[0] + s[1]) + (s[2] + s[3])) * (1.f / D_);
#pragma unroll
  for (int i = 0; i < 16; i++) { f[i] -= m; q[i & 3] = i < 4 ? f[i] * f[i] : q[i & 3] + f[i] * f[i]; }
  const float r = __builtin_amdgcn_rsqf(wave_sum((q[0] + q[1]) + (q[2] + q[3])) * (1.f / D_) + 1e-5f);
#pragma unroll
  for (int h = 0; h < 2; h++) {
    u32x4 o;
#pragma unroll
    for (int i = 0; i < 4; i++) o[i] = pack2(f[8 * h + 2 * i] * r, f[8 * h + 2 * i + 1] * r);
    row[h * 64] = o;
  }
}
#define NA 3
#define NC (16 - NA)
#define NMI 0
#define V1 1
#define V2 0
#define V3 0
#define SL3 6
#define BE 1
#define PG 2
#define XS 0
#define WE 1
#define TCE 0
#define TC0 640
#define WNT ""
#define XBAR 1
#define NTC 2
struct T1 {
  int cl;
  u32 vw[4];
  const char* Wb;
  DI T1(const Ctx& c, int lane) {
    const int tw = c.xcd * 4 + c.ri, rr = lane >> 3;
    cl = (c.ri + c.rj) & 3;
    Wb = (tw < 16 ? c.wsr : c.wsi) + (size_t)(tw & 15) * 128 * 4096;
#pragma unroll
    for (int i = 0; i < 4; i++) vw[i] = (((c.w & 3) * 4 + i) * 8 + rr) * 4096 + (((lane & 7) ^ rr) << 4) - i * 1024;
  }
  DI void w(const Ctx& c, int q) const { glds4(Wb + kstep<KMP>(q, cl, 32) * 128, (q & 3) * STAGE + WO + (c.w & 3) * 4096, vw[0], vw[1], vw[2], vw[3]); }
};
DI void k0_mma(const Ctx& c, Dg& dg) {
  const int lane = lane_id();
  const T1 t(c, lane);
  t.w(c, 0);
  if (WE == 2) t.w(c, 2);
  if (c.w == 0 && lane < 8) ((u32*)(c.smem + AUX))[768 + lane] = 0;
  WAIT_V(WE == 2 ? 8 : 4);
  ln_row(c, lane);
  STAMP(20);
  LBAR();
  STAMP(21);
  BAR();
  auto touch = [&]() {
#pragma unroll
    for (int i = 0; i < 4 * NTC; i++) stouch2(t.Wb + (c.rj * 16 + c.w * 4 + (i & 3)) * 4096 + TC0 + (i >> 2) * 256);
  };
  if (WE == 1) t.w(c, 2);
  if (WE) t.w(c, 3);
  if (TCE) touch();
  if (!WE || !TCE) {
    u32 a = AUX + 772 * 4, v;
    asm volatile("1:\n\tds_read_b32 %0, %1\n\ts_waitcnt lgkmcnt(0)\n\tv_cmp_gt_u32 vcc, 4, %0\n\ts_and_b64 vcc, vcc, exec\n\ts_cbranch_scc1 1b\n\ts_sleep 1"
                 : "=&v"(v) : "v"(a) : "vcc", "scc", "memory");
  }
  STAMP(24);
  if (!WE) { t.w(c, 2); t.w(c, 3); }
  if (!TCE) touch();
  WAIT_V(0);
  STAMP(22);
  static_assert(BE || !XS, "");
}

DI void g1_load(const Ctx& c, Sk<2>& k2, Dg& dg) {
  constexpr int N = 32;
  const int lane = lane_id(), lw = c.w - 4;
  const rsrc_t rw = mkrsrc(c.ws);
  const int ks = c.b & 7, cb = c.b >> 3, r0 = ks * 128 + (lane >> 3) * 16;
  u32x4 cv[2], wv[16], af[16];
  u32x2 ep;
  asm volatile("global_load_dwordx4 %0, %3, %4\n\tglobal_load_dwordx4 %1, %3, %4 offset:16\n\tglobal_load_dwordx2 %2, %5, %6"
               : "=&v"(cv[0]), "=&v"(cv[1]), "=&v"(ep) : "v"(r0 * 2), "s"(c.cv), "v"((u32)WS_EP), "s"(c.ws) : "memory");
  const T1 t(c, lane);
  t.w(c, 1);
  u32 wo = (r0 * 12288 + cb * 384 + max(lw - 1, 0) * 128 + (lane & 7) * 16) & -min(lw + V2, 1);
  auto wload = [&](int i0, int i1) {
#pragma unroll
    for (int i = i0; i < i1; i++)
      asm volatile("global_load_dwordx4 %0, %1, %2" WNT : "=v"(wv[i]) : "v"(wo), "s"(c.wm + i * 12288) : "memory");
  };
  wload(0, NA);
  WAIT_V(7 + NA);
  ln_row(c, lane);
  STAMP(20);
  LBAR();
  STAMP(21);
  {
    const u32 a = SM + ((lane >> 2) & 3) * SMS + lw * 512 + (lane >> 5) * 64 + (lane & 3) * 16 + ((lane >> 4) & 1) * 8;
    const u32 o = WS_NT + (lw * 2 * N + (c.b >> 3)) * 16384 + (lane >> 4) * 2048 + (c.b & 7) * 256 + (lane & 15) * 16;
#pragma unroll
    for (int h = 0; h < 4; h++)
      st16(rw, o + (h >> 1) * N * 16384 + (h & 1) * 8192, __builtin_bit_cast(u32x4, lds_tr2(a + h * 128, a + h * 128 + 4 * SMS)));
  }
  SB();
  {
    static_assert(FLS == 128, "");
    const u32 o1 = lw & 2 ? WS_ST + c.b * 1024 : WS_MODP + ((c.b & 7) * 6144 + (c.b >> 3) * 192) * 4;
    u32 o = (lw & 1 ? o1 : WS_FL + c.b * 512) + lane * 16;
    if (lane >= (lw & 1 ? 40 + lw * 8 : lw * 16)) o = 0x7ffffff0u;
    st16(rw, o, u32x4{EMPTY, EMPTY, EMPTY, EMPTY});
  }
  SB();
  BAR();
  u32 mo = lane & 40 || !lw ? 0x7ffffff0u : WS_MODP + (ks * 6144 + cb * 192 + (lw - 1) * 64) * 4 + (lane & 7) * 32 + (lane & 16);
  wload(NA, NA + NMI);
  pin(mo);
  WAIT_V(9 + NA + NMI);
  asm volatile("" : "+v"(cv[0]), "+v"(cv[1]), "+v"(ep));
  {
    u32x4 mk;
#pragma unroll
    for (int i = 0; i < 4; i++) mk[i] = (lane & 6) == 2 * i ? 0xffffu << (lane & 1) * 16 : 0;
#pragma unroll
    for (int i = 0; i < 16; i++) {
      const u32 v = cv[i >> 3][(i >> 1) & 3];
      const float f = i & 1 ? bf_hi(v) : bf_lo(v), s = f * __builtin_amdgcn_rcpf(1.f + __expf(-f));
      af[i] = mk & pack2(s, s);
      pin(af[i]);
    }
  }
  f32x4 md = {0.f, 0.f, 0.f, 0.f};
  auto gemv = [&](int i) {
    pin(wv[i]);
    md = mfma16(__builtin_bit_cast(bf16x8, af[i]), __builtin_bit_cast(bf16x8, wv[i]), md);
    pin(md);
  };
  const int cl = t.cl;
  const u32 vx = lane * 16 + lw * 4096;
  const char* X = c.ws + WS_NT + (size_t)c.rj * N * 16384;
  auto issue = [&](int q, int wx) {
    if (wx & 1) t.w(c, q);
    if (wx & 2) glds4_dev(X + (size_t)kstep<KMP>(q, cl, N) * 16384, (q & 3) * STAGE + lw * 4096, vx);
  };
  const u64 tag = ((u64)(u32)__builtin_amdgcn_readfirstlane(ep[1]) << 32 | (u32)__builtin_amdgcn_readfirstlane(ep[0])) + 0x9e3779b97f4a7c15ul;
  const char *p0 = X, *p1 = X + (size_t)kstep<KMP>(1, cl, N) * 16384;
  u32 fo = WS_F0 + (c.b & (REP - 1)) * 2048 + lw * 512 + lane * 8;
  u64 f = ~tag;
  u32 tab = kstep<KMP>(lane & 31, cl, N);
  asm volatile("" : "+s"(p0), "+s"(p1), "+v"(fo), "+v"(f), "+v"(tab));
  if (last_of_4<NMI>(4) && lane < REP)
    __builtin_amdgcn_raw_buffer_store_b64(__builtin_bit_cast(u32x2, tag), rw, WS_F0 + lane * 2048 + c.b * 8, 0, 16);
  STAMP(22);
  wload(NA + NMI, NA + NC);
#pragma unroll
  for (int i = 0; i < NA; i++) gemv(i);
  k2.init(lane);
  constexpr int NL = 16 - NA - NC;
  const char *p2 = X + (size_t)kstep<KMP>(2, cl, N) * 16384, *p3 = X + (size_t)kstep<KMP>(3, cl, N) * 16384;
  pins(p2); pins(p3);
  auto look = [&]() { asm volatile("global_load_dwordx2 %0, %1, %2 sc1" : "+&v"(f) : "v"(fo), "s"(c.ws) : "memory"); };
  auto rest = [&]() {
    glds4_dev(p2, 2 * STAGE + lw * 4096, vx);
    glds4_dev(p3, 3 * STAGE + lw * 4096, vx);
    if (XS) BAR();
    wload(NA + NC, 16);
  };
  u32x4 mv;
  auto fold = [&](int e) {
    const float m = md[e];
    auto h = __builtin_amdgcn_permlane32_swap(f2u(m), f2u(m), false, false);
    u32 v = f2u(m + dpp<0x128>(u2f(h[1])));
    v = v == EMPTY ? 0x7fc00000u : v;
    pin(v);
    mv[e] = v;
  };
  if (lw == 0) await(c, fo, f, tag);
  if (V3) for (int i = V3 == 1 ? c.ri : V3 == 2 ? c.xcd & 3 : c.rj >> 1; i > 0; i--) __builtin_amdgcn_s_sleep(SL3);
  release(773, lw);
  STAMP(23);
  glds4_dev(p0, lw * 4096, vx);
  glds4_dev(p1, STAGE + lw * 4096, vx);
  look();
  if (BE) rest();
  if (PG == 2) sfor<NA, 16>([&](auto ic) {
    constexpr int i = decltype(ic)::value;
    WAIT_V((BE ? 32 : 24) - i);
    gemv(i);
  });
  if (V1) { fold(0); fold(1); fold(2); fold(3); st16(rw, mo, mv); SB(); }
  WAIT_V(5 + (BE ? 8 + NL : 0) + V1);
  BAR();
  STAMP(1);
  const int gw = c.b * 4 + lw;
  const char* t0 = (gw & 1 ? c.wdi : c.wdr) + (gw >> 1) * 256;
  const char* t1 = (gw & 1 ? c.wu : c.wg) + (gw >> 1) * 256 - 16 * 131072;
  if (!BE) rest();
  stouch2(t0);
  WAIT_V(9 + NL + V1);
  BAR();
  issue(4, 3);
  stouch2(t0 + 131072);
  const char* pw = t.Wb + kstep<KMP>(5, cl, N) * 128;
  pins(pw);
  setm0<STAGE + WO>(lw * 4096);
  WAIT_V(12 + NL + V1 * BE);
  if (lw == 1) await(c, fo, f, tag);
  BAR();
  const u32 mb = lw * 4096;
  sfor<2, N - 3>([&](auto jc) {
    constexpr int j = decltype(jc)::value, q = j + 3;
    dma<0, 0>(pw, t.vw[0]);
    const char* px = X + ((size_t)(u32)__builtin_amdgcn_readlane(tab, q) << 14);
    pins(px);
    dma<1024, 0>(pw, t.vw[1]);
    stouch2((j < 16 ? t0 : t1) + j * 131072);
    dma<2048, 0>(pw, t.vw[2]);
    dma<3072, 0>(pw, t.vw[3]);
    setm0<(q & 3) * STAGE>(mb);
    dma<0, 1>(px, vx);
    pw = t.Wb + ((size_t)(u32)__builtin_amdgcn_readlane(tab, q + 1) << 7);
    pins(pw);
    dma<1024, 1>(px, vx);
    dma<2048, 1>(px, vx);
    dma<3072, 1>(px, vx);
    if (FE && j == N - 5) {
      glds1<0, 1>(c.ws + WS_MODP, G3_WA + mb, k2.vm);
      glds1<1024, 1>(c.ws + WS_MODP + 4 * 24576 - 1024, G3_WA + mb, k2.vm);
    }
    setm0<((q + 1) & 3) * STAGE + WO>(mb);
    if (j == 4) look();
    constexpr int JE = V1 ? -9 : PG == 2 ? 2 : PG ? 4 : 4 + (17 - NA) / 2;
    auto ex = [](int i) constexpr { return (i == 4) + (i == JE + 4) + (FE && i == N - 5 ? 2 : 0); };
    if (PG == 1 && j == 3) sfor<NA, 16>([&](auto ic) {
      constexpr int i = decltype(ic)::value;
      if (i >= NA + NC) WAIT_V(39 - i);
      gemv(i);
    });
    if (!PG && j >= 4 && j < JE) {
      gemv(NA + (j - 4) * 2);
      if (NA + (j - 4) * 2 + 1 < 16) gemv(NA + (j - 4) * 2 + 1);
    }
    if (j >= JE && j < JE + 4) fold((j - JE) & 3);
    if (j == JE + 4) {
      st16(rw, mo, mv);
      SB();
    }
    WAIT_V(16 + ex(j - 2) + ex(j - 1) + ex(j) + (j == 2 ? NL + V1 * BE : 0));
    if (j == 7) await(c, fo, f, tag);
    BAR();
  });
  if (c.b == 0 && lw == 0 && lane == 0) __builtin_amdgcn_raw_buffer_store_b64(__builtin_bit_cast(u32x2, tag), rw, WS_EP, 0, 16);
}

DI void g1_mma(const Ctx& c, u32x4 (&x2)[4], Dg& dg) {
  constexpr int N = 32;
  const int lane = lane_id(), wx = c.w >> 1, ww = c.w & 1, g = lane >> 4, cc = lane & 15;
  const int tw = c.xcd * 4 + c.ri, tx = c.rj;
  u32 xa = wx * 4096 + lane * 16, wa[4];
#pragma unroll
  for (int i = 0; i < 4; i++)
    wa[i] = WO + ww * 8192 + ((i & 1) ^ wx) * 4096 + cc * 128 + ((((i >> 1) * 4 + g) ^ (cc & 7)) << 4);
  f32x4 acc[4][4], rsum[2];
#pragma unroll
  for (int i = 0; i < 18; i++) {
    f32x4& v = i < 16 ? acc[i >> 2][i & 3] : rsum[i & 1];
    v = f32x4{0.f, 0.f, 0.f, 0.f};
    pin(v);
  }
  bf16x8 fx[2][4], fw[2][4];
  auto rd = [&](int kk, u32 so) {
#pragma unroll
    for (int n = 0; n < 4; n++) fw[kk][n] = lds_b128(wa[kk * 2 + (n >> 1)] + so + (n & 1) * 2048);
#pragma unroll
    for (int m = 0; m < 4; m++) fx[kk][m] = lds_b128(xa + so + (m >> 1) * 8192 + (m & 1) * 2048 + kk * 1024);
  };
  u32x4 o4 = {0x3f803f80u, 0x3f803f80u, 0x3f803f80u, 0x3f803f80u};
  pin(o4);
  const bf16x8 ones = __builtin_bit_cast(bf16x8, o4);
  u32 ea[6] = {AUX + (512 + ww * 64 + wx * 32 + (lane & 31)) * 4u, AUX + (256 + wx * 32 + g * 8) * 4u};
#pragma unroll
  for (int i = 0; i < 2; i++) {
    ea[2 + i] = AUX + (512 + ww * 64 + (i ^ wx) * 32 + cc) * 4;
    ea[4 + i] = ww * 8192 + (i ^ wx) * 4096 + wx * 1024 + lane * 16;
  }
#pragma unroll
  for (int i = 0; i < 6; i++) pin(ea[i]);
  u32 pp = WS_PP + ((tw & 15) * 32 + (tw >> 4) * 16 + tx * 2) * 16384;
  asm volatile("" : "+s"(pp));
  auto mm = [&](int kk) {
#pragma unroll
    for (int m = 0; m < 4; m++)
#pragma unroll
      for (int i = 0; i < 4; i++) {
        const int n = (m & 1) ? 3 - i : i;
        acc[m][n] = mfma16(fx[kk][m], fw[kk][n], acc[m][n]);
      }
    rsum[0] = mfma16(ones, fw[kk][0], rsum[0]);
    rsum[1] = mfma16(ones, fw[kk][1], rsum[1]);
    sched_rm<0, 18>();
  };
  auto resid = [&]() {
    const rsrc_t rx = mkrsrc(c.x, S_ * D_ * 2);
    const u32 o = ((tw & 15) * 128 + wx * 64 + cc) * 2048 + (tx * 128 + (tw >> 4) * 64 + ww * 32 + (g & 1) * 16 + (g >> 1) * 8) * 2;
#pragma unroll
    for (int m = 0; m < 4; m++) x2[m] = __builtin_amdgcn_raw_buffer_load_b128(rx, o + m * 32768, 0, 0);
  };
  if (X2E == 1) resid();
  pin(xa); pin(wa[0]); pin(wa[1]); pin(wa[2]); pin(wa[3]);
  TL(1);
  if (XS) BAR();
  LBAR(); TL(1);
  STAMP(1);
  rd(0, 0);
  if (X2E == 2) resid();
  SB();
  for (int k = N;;) {
    WAIT_L(); SB();
    rd(1, 0);
    mm(0);
    LBAR(); TL(1); SB();
    rd(0, STAGE);
    mm(1);
    WAIT_L(); SB();
    rd(1, STAGE);
    xa ^= 2 * STAGE; wa[0] ^= 2 * STAGE; wa[1] ^= 2 * STAGE; wa[2] ^= 2 * STAGE; wa[3] ^= 2 * STAGE;
    k -= 2;
    mm(0);
    pin(xa); pin(wa[0]); pin(wa[1]); pin(wa[2]); pin(wa[3]);
    asm volatile("" : "+s"(k));
    if (k == 0) break;
    LBAR(); TL(1); SB();
    rd(0, 0);
    mm(1);
  }
  LBAR(); TL(1); SB();
  auto last = [&](int a, int b) {
#pragma unroll
    for (int i = a; i < b; i++) {
      const int m = (i >> 3) * 2 + (i & 1), n = (i >> 1) & 3;
      acc[m][n] = mfma16(fx[1][m], fw[1][n], acc[m][n]);
    }
#pragma unroll
    for (int i = a; i < b; i++) {
      __builtin_amdgcn_sched_group_barrier(0x008, 1, 0);
      __builtin_amdgcn_sched_group_barrier(0x126, 3, 0);
    }
    SB();
  };
  rsum[0] = mfma16(ones, fw[1][0], rsum[0]);
  rsum[1] = mfma16(ones, fw[1][1], rsum[1]);
  SB();
  last(0, 2);
  *(LDS_AS float*)(size_t)ea[0] = ea[0] & 64 ? rsum[1][0] : rsum[0][0];
  SB();
  if (!X2E) resid();
  last(2, 6);
  LBAR(); TL(1); SB();
  STAMP(2);
  const rsrc_t rw = mkrsrc(c.ws);
  f32x4 sa[4], ca[4];
  float rs[4];
#pragma unroll
  for (int i = 0; i < 4; i++) {
    const u32 d0 = ea[1] + (i >> 1) * 256 + (i & 1) * 16;
    rs[i] = *(LDS_AS float*)(size_t)(ea[2 + (i >> 1)] + (i & 1) * 64);
    sa[i] = *(LDS_AS f32x4*)(size_t)d0;
    ca[i] = *(LDS_AS f32x4*)(size_t)(d0 + 512);
  }
  last(6, 16);
  u32x4 late[4];
#pragma unroll
  for (int i = 0; i < 8; i++) {
    const int h = i >> 2, n = i & 3;
    const u32 to = ea[4 + (n >> 1)] + (n & 1) * 2048;
    const f32x4 a = acc[2 * h][n] * ca[2 * h] + sa[2 * h] * rs[n], bb = acc[2 * h + 1][n] * ca[2 * h + 1] + sa[2 * h + 1] * rs[n];
    u32x4 o = {pack2(a[0], a[1]), pack2(a[2], a[3]), pack2(bb[0], bb[1]), pack2(bb[2], bb[3])};
    if (i < 4 + H1E) __builtin_amdgcn_raw_buffer_store_b128(o, rw, to, pp + h * 16384, 16);
    else late[n] = o;
    *(LDS_AS u32x4*)(size_t)(to + h * STAGE) = o;
    SB();
  }
  publish<H1E>(c, 3, lane, 8, WS_FL + ((tw & 15) * 16 + lane * 2 + (tw >> 4)) * FLS + 64 + tx * 4);
#pragma unroll
  for (int n = H1E; n < 4; n++) __builtin_amdgcn_raw_buffer_store_b128(late[n], rw, ea[4 + (n >> 1)] + (n & 1) * 2048, pp + 16384, 16);
}

template <int G>
DI void sk_load(const Ctx& c, Sk<G>& k, const u32x4* pk, u32 po, Dg& dg) {
  const int lane = G == 2 ? k.ln : lane_id();
  constexpr int S = Sk<G>::S, NQ = Sk<G>::NQ;
  const int tx = k.tx, tw = k.tw, lw = k.lw;
  float* aux = (float*)(c.smem + AUX);
  const rsrc_t rw = mkrsrc(c.ws);
  const u32 inbox = WS_FL + (G - 1) * FLK + (tx * 16 + tw) * FLS + ((G == 4 ? (tw & 1) * 16 : 0) + (lane & 15)) * 4;
  const int gw = c.b * 4 + lw;
  const char* t0 = (gw & 1 ? c.wu : c.wg) + (gw >> 1) * 256 + 11 * 131072;
  auto touch = [&](int j) {
    if (G == 2) stouch2(t0 + j * 131072);
  };
  if (G == 2) {
    const u32 raw = G3_WA + lw * 4096;
    auto fetch = [&](int h, u32 v) {
      glds1<0, 1>(c.ws + WS_MODP, raw + h * 2048, v);
      glds1<1024, 1>(c.ws + WS_MODP + 4 * 24576 - 1024, raw + h * 2048, v);
    };
    auto sum = [&](int h, u32 v) {
      float q[8];
#pragma nounroll
      for (int it = 0; it < SPIN_CAP; it++) {
        u32 mx = 0;
#pragma unroll
        for (int i = 0; i < 8; i++) {
          q[i] = ((const float*)(c.smem + raw))[h * 512 + i * 64 + lane];
          mx = max(mx, f2u(q[i]));
        }
        if (!__builtin_amdgcn_ballot_w64(mx == EMPTY)) break;
        fetch(h, v);
        WAIT_V(0);
      }
      const float s = rbf(((q[0] + q[1]) + (q[2] + q[3])) + ((q[4] + q[5]) + (q[6] + q[7])));
      const float u = (h ? lw == 2 : lw >= 2) ? rbf(1.f + s) : s;
      aux[(h ? 0 : 256) + lw * 64 + lane] = (h | ~tw) & 1 ? u : -u;
    };
    if (!FE) fetch(0, k.vm);
    k.template issue<true, 1>(0);
    WAIT_V(14);
    BAR();
    k.template issue<true, 1>(1);
    const u32 v1 = (lane >> 4) * 24576 + (lane & 15) * 16 + (2048 + lw * 1024 + tw * 64) * 4;
    fetch(1, v1);
    WAIT_V(FE ? 10 : 12);
    BAR();
    k.template issue<true, 1>(2);
    WAIT_V(14);
    sum(0, k.vm);
    LBAR();
    if (W3E) k.template issue<true, 1>(3);
    BAR();
    if (!W3E) k.template issue<true, 1>(3);
    STAMP(2);
    WAIT_V(8);
    sum(1, v1);
    {
      const char *p0 = k.X + (size_t)k.blk(2) * 16384, *p1 = k.X + (size_t)k.blk(3) * 16384;
      u32 v = k.vx, f = EMPTY;
      asm volatile("" : "+s"(p0), "+s"(p1), "+v"(v));
      LBAR();
      if (!EP) BAR();
      STAMP(3);
      if (lw == 0) await(c, WS_FL + (tx * 16 + tw) * FLS + 64 + (lane & 7) * 4, f);
      release(775, lw);
      STAMP(4);
      glds4_dev(p0, 2 * STAGE + lw * 4096, v);
      if (XE3) glds4_dev(p1, 3 * STAGE + lw * 4096, v);
      if (EP) BAR();
      if (!XE3) glds4_dev(p1, 3 * STAGE + lw * 4096, v);
    }
    k.template issue<true>(4);
    WAIT_V(12);
    BAR();
    STAMP(5);
    u32 flag;
#pragma unroll
    for (int j = 2; j < 6; j++) {
      if (j == 4) flag = peek(c, inbox);
      k.template issue<true>(j + 3);
      touch(j);
      if (j == 4) WAIT_V(17);
      else WAIT_V(16);
      BAR();
    }
    await(c, inbox, flag);
  } else {
    auto ns = [](int j) constexpr { return j < 0 || j > JL ? 0 : j < JL ? NS : 8 - NE - JL * NS; };
    u32 flag;
    WAIT_V(NE);
    STAMP(15);
    BAR();
    STAMP(17);
    sfor<0, 9>([&](auto jc) {
      constexpr int j = decltype(jc)::value;
      if (j == JP) flag = peek(c, inbox);
      k.template issue<true>(j + 3);
#pragma unroll
      for (int i = NE + j * NS; i < NE + j * NS + ns(j); i++) st16(rw, po + i * 1024, pk[i]);
      SB();
      if (j >= 2 || j == JF) WAIT_V(j == 8 && JP == 8 ? 8 : 16 + (j == JF ? 0 : ns(j - 2) + ns(j - 1) + ns(j)) + (j == JP));
      BAR();
    });
    await(c, inbox, flag);
  }
#pragma nounroll
  for (int j = G == 2 ? 6 : 9; j < S - 3; j++) {
    k.template issue<true>(j + 3);
    touch(j);
    WAIT_V(16);
    BAR();
  }
  u32 flag;
  if (G == 4) flag = peek(c, inbox ^ 64);
  k.template issue<false>(S);
  touch(S - 3);
  WAIT_V(14 + (G == 4));
  BAR();
  k.template issue<false>(S + 1);
  touch(S - 2);
  WAIT_V(12);
  BAR();
  if (G == 4) await(c, inbox ^ 64, flag);
#pragma nounroll
  for (int j = S - 1; j < NQ - XB; j++) {
    k.template issue<false>(j + 3);
    touch(j);
    WAIT_V(12);
    BAR();
  }
  const u32 fl = WS_FL + c.b * FLS + (G == 2 ? 0 : 8) + (lw >> 1) * 4;
  flag = peek(c, fl);
  auto part = [&]() {
    await(c, fl, flag);
    glds4_dev(c.ws + Sk<G>::XBUF + (size_t)c.b * 16384, Sk<G>::PT + lw * 4096, k.vx);
  };
  static_assert(XB >= 5 + PE && PE <= 2 && (!PE || (W3P == 2 && !W3A)), "");
#pragma unroll
  for (int j = NQ - XB; j < NQ - 3; j++) {
    k.template issue<false>(j + 3);
    touch(j);
    if (j == NQ - 3 - PE) part();
    if (j < NQ - XB + 2 && j < NQ - 4 - PE) WAIT_V(13);
    else if (j >= NQ - 3 - PE) WAIT_V(16);
    else WAIT_V(12);
    BAR();
  }
  if (!PE) part();
  WAIT_V(10);
  BAR();
  if (G == 4) {
    WAIT_V(PE == 2 ? 0 : 4);
    BAR();
    WAIT_V(0);
    BAR();
    return;
  }
  static_assert(G != 2 || (NQ & 3) == 2, "");
  const T3 t(c);
  u32 vo[5];
#pragma unroll
  for (int i = 0; i < 5; i++) { vo[i] = i < 3 ? t.aoff(i, lane) : t.boff(i - 3, lane); pin(vo[i]); }
  if (W3A) {
    glds3(t.asrc(c, t.ks(0)), t.adst(0), vo[0], vo[1], vo[2]);
    glds3(t.asrc(c, t.ks(1)), t.adst(1), vo[0], vo[1], vo[2]);
  }
  WAIT_V((PE == 2 ? 0 : 4) + 6 * W3A);
  BAR();
  WAIT_V(6 * W3A);
  BAR();
  auto w3 = [&](int q) {
    if (!W3A) glds3(t.asrc(c, t.ks(q)), t.adst(q), vo[0], vo[1], vo[2]);
    glds2(t.bsrc(c.wg, t.ks(q)), t.bdst(q), vo[3], vo[4]);
    glds2(t.bsrc(c.wu, t.ks(q)), t.bdst(q) + 8192, vo[3], vo[4]);
  };
  if (W3P < 2 || W3P == 3) w3(0);
  if (W3P < 1) w3(1);
  STAMP(6);
  const int row = lw * 32 + (lane & 31);
  u32 go = WS_ST + ((tx * 16 + (lane >> 5) * 8) * 128 + row) * 8, so = WS_ST + ((tx * 16 + tw) * 128 + row) * 8, sa = AUX + 1024 + row * 8;
  if (lane >= 32) so = 0x7ffffff0u;
  pin(go); pin(so); pin(sa);
  BAR();
  {
    const f32x2 p = *(const LDS_AS f32x2*)(size_t)sa, q = *(const LDS_AS f32x2*)(size_t)(sa + 1024);
    const float d = p[0] - q[0];
    u32 m = f2u(0.5f * (p[0] + q[0])), v = f2u(p[1] + q[1] + d * d * 16.f);
    m = m == EMPTY ? 0x7fc00000u : m;
    v = v == EMPTY ? 0x7fc00000u : v;
    __builtin_amdgcn_raw_buffer_store_b64(u32x2{m, v}, rw, so, 0, 16);
    SB();
  }
  STAMP(7);
  if (W3P == 2) w3(0);
  if (W3P == 1 || W3P == 2) w3(1);
  if (SL7) __builtin_amdgcn_s_sleep(SL7);
  u32 gm[8], gq[8];
#pragma unroll
  for (int i = 0; i < 8; i++) gm[i] = gq[i] = EMPTY;
  for (int it = 0; it < SPIN_CAP; it++) {
    asm volatile("" ::: "memory");
    u32x2 nv[8];
#pragma unroll
    for (int i = 0; i < 8; i++)
      nv[i] = __builtin_amdgcn_raw_buffer_load_b64(rw, SP || max(gm[i], gq[i]) == EMPTY ? go + (i & 3) * 1024 : 0x7ffffff0u, (i >> 2) * 4096, 16);
    SB();
    u32 mx = 0;
#pragma unroll
    for (int i = 0; i < 8; i++) {
      const bool miss = SP || max(gm[i], gq[i]) == EMPTY;
      const u32 a0 = nv[i][0], a1 = nv[i][1];
      gm[i] = miss ? a0 : gm[i];
      gq[i] = miss ? a1 : gq[i];
      mx = max(mx, max(gm[i], gq[i]));
    }
    if (!__builtin_amdgcn_ballot_w64(mx == EMPTY)) break;
  }
  float sm = 0.f, q = 0.f;
#pragma unroll
  for (int i = 0; i < 8; i++) sm += u2f(gm[i]);
  sm = sum32(sm) * (1.f / 16);
#pragma unroll
  for (int i = 0; i < 8; i++) {
    const float d = u2f(gm[i]) - sm;
    q += u2f(gq[i]) + 64.f * d * d;
  }
  q = sum32(q);
  q = __builtin_amdgcn_rsqf(q * (1.f / D_) + 1e-5f);
  *(LDS_AS f32x2*)(size_t)sa = f32x2{-sm * q, q};
  LBAR();
  STAMP(8);
  if (W3P == 3) w3(1);
}

template <int G>
DI void sk_mma(const Ctx& c, const Sk<4>& k4, u32x4 (&x2)[4], Dg& dg) {
  constexpr int S = Sk<G>::S, OWN = Sk<G>::OWN;
  const int lane = lane_id(), wx = c.w >> 1, ww = c.w & 1, g = lane >> 4, cc = lane & 15;
  const int tx = (c.xcd & 3) * 4 + c.ri, tw = c.rj * 2 + (c.xcd >> 2), pb = c.b ^ 4;
  const rsrc_t rw = mkrsrc(c.ws);
  u32 xa = wx * 8192 + lane * 16;
  u32 wa[4];
  if (G == 2) {
#pragma unroll
    for (int i = 0; i < 4; i++)
      wa[i] = WO + ww * 8192 + ((i & 1) ^ ww) * 4096 + cc * 128 + ((((i >> 1) * 4 + g) ^ (cc & 7)) << 4);
  } else {
    const int q = cc >> 2, p = lane & 3;
    const int sw = ((q >> 1) | ((g & 1) << 1)) ^ (ww << 1);
#pragma unroll
    for (int n = 0; n < 4; n++) wa[n] = WO + ww * 8192 + (g * 8 + q) * 128 + ((n ^ sw) << 5) + p * 8;
  }
  f32x4 acc[4][4];
#pragma unroll
  for (int i = 0; i < 16; i++) {
    acc[i >> 2][i & 3] = f32x4{0.f, 0.f, 0.f, 0.f};
    pin(acc[i >> 2][i & 3]);
  }
  bf16x8 fx[2][4], fw[2][4];
  auto rd = [&](int bf, int kk, u32 so) {
#pragma unroll
    for (int n = 0; n < 4; n++) {
      if (G == 2) fw[bf][n] = lds_b128(wa[kk * 2 + (n >> 1)] + so + (n & 1) * 2048);
      else fw[bf][n] = lds_tr2(wa[n] + so + kk * 4096, wa[n] + so + kk * 4096 + 512);
    }
#pragma unroll
    for (int m = 0; m < 4; m++) fx[bf][m] = lds_b128(xa + so + m * 2048 + kk * 1024);
  };
  auto mm = [&](int bf) {
#pragma unroll
    for (int m = 0; m < 4; m++)
#pragma unroll
      for (int i = 0; i < 4; i++) {
        const int n = (m & 1) ? 3 - i : i;
        acc[m][n] = mfma16(fw[bf][n], fx[bf][m], acc[m][n]);
      }
    sched_rm<G == 2 ? 0 : 4, 16>();
  };
  auto flip = [&](u32 mx, u32 mw) { xa ^= mx; wa[0] ^= mw; wa[1] ^= mw; wa[2] ^= mw; wa[3] ^= mw; };
  pin(xa); pin(wa[0]); pin(wa[1]); pin(wa[2]); pin(wa[3]);
  f32x2 f[4][4];
#pragma unroll
  for (int i = 0; i < 16; i++) {
    f[i >> 2][i & 3] = f32x2{bf_lo(x2[i >> 2][i & 3]), bf_hi(x2[i >> 2][i & 3])};
    pin(f[i >> 2][i & 3]);
  }
  const int e0 = ww * 32 + (g & 1) * 16 + (g >> 1) * 8;
  u32 ea[5] = {(wx ? Sk<G>::EX1 : Sk<G>::EX0) + ww * 8192 + lane * 16u, Sk<G>::PT + c.w * 4096 + lane * 16u, AUX + e0 * 4u,
               G == 2 ? AUX + (256 + ww * 256 + (wx * 64 + lane) * 2) * 4u : (tx * 128 + wx * 64 + cc) * 2048 + (tw * 64 + e0) * 2u};
  ea[4] = ea[0] ^ 8192;
#pragma unroll
  for (int i = 0; i < 5; i++) pin(ea[i]);
  auto ld4 = [](u32 a) { return *(const LDS_AS f32x4*)(size_t)a; };
  auto half = [](f32x4 v, int h) { return f32x2{v[2 * h], v[2 * h + 1]}; };
  TL(G);
  STAMP(G == 2 ? 3 : 15);
  if (G == 4) {
    const char *p0 = k4.X + (size_t)k4.blk(0) * 16384, *p1 = k4.X + (size_t)k4.blk(1) * 16384;
    u32 v = k4.vx, go = AUX + 774 * 4, f = EMPTY;
    asm volatile("" : "+s"(p0), "+s"(p1), "+v"(v), "+v"(go));
    if (c.w == 0) {
      await(c, WS_FL + FLK + (tx * 16 + tw) * FLS + 64 + cc * 4, f);
      lds_inc(774);
    }
    asm volatile("1:\n\tds_read_b32 %0, %1\n\ts_waitcnt lgkmcnt(0)\n\tv_cmp_gt_u32 vcc, 5, %0\n\ts_and_b64 vcc, vcc, exec\n\ts_cbranch_scc0 2f\n\ts_sleep 1\n\ts_branch 1b\n2:"
                 : "=&v"(f) : "v"(go) : "vcc", "scc", "memory");
    STAMP(16);
    glds4_dev(p0, c.w * 4096, v);
    glds4_dev(p1, STAGE + c.w * 4096, v);
    k4.issue<true>(2);
    glds_dw(c.wd, AUX + 3328, lane * 4);
    WAIT_V(13);
  }
  const T3 t3(c);
  u32 lf = WS_FL + 3 * FLK + (t3.tm * 32 + lane) * FLS + t3.tn * 4;
  if (G == 4) {
    if (c.w || lane >= 32) lf = 0x7ffffff0u;
    pin(lf);
  }
  LBAR(); TL(G);
  if (G == 4) STAMP(17);
  rd(0, 0, 0);
  SB();
  static_assert(S % 2 == 0 && OWN % 2 == 0, "");
  int k = S / 2 - 1, ke = G == 2 ? 0 : k - JF / 2;
#pragma nounroll
  for (;; ke = 0) {
#pragma nounroll
   for (; k >= ke; k--) {
    WAIT_L(); SB();
    rd(1, 1, 0);
    mm(0);
    if (G == 4) WAIT_V(9);
    LBAR(); TL(G); SB();
    rd(0, 0, STAGE);
    mm(1);
    WAIT_L(); SB();
    rd(1, 1, STAGE);
    const u32 t = k ? 0 : ww;
    flip(2 * STAGE | t * 1024, 2 * STAGE | t * (G == 2 ? 8192 + 64 : 8192 + 4096));
    mm(0);
    pin(xa); pin(wa[0]); pin(wa[1]); pin(wa[2]); pin(wa[3]);
    if (G == 2 && k == S / 2 - 1) {
      publish(c, 0, lane, 16, WS_FL + FLK + (tx * 16 + lane) * FLS + ((tw & 1) * 8 + c.rj) * 4);
      STAMP(4);
    }
    if (G == 4) WAIT_V(1);
    LBAR(); TL(G); SB();
    rd(0, 0, 0);
    mm(1);
   }
   if (k < 0) break;
   __builtin_amdgcn_raw_buffer_store_b32(1u, rw, lf, 0, 16);
  }
  if (ww) {
#pragma unroll
    for (int m = 0; m < 4; m++)
#pragma unroll
      for (int h = 0; h < 2; h++) {
        const f32x4 v = acc[m][2 * h], u = acc[m][2 * h + 1];
        st16(rw, Sk<G>::XBUF + pb * 16384 + wx * 8192 + ((h ^ 1) * 4 + m) * 1024 + lane * 16,
             u32x4{pack2(v[0], v[1]), pack2(v[2], v[3]), pack2(u[0], u[1]), pack2(u[2], u[3])});
        acc[m][2 * h] = acc[m][2 * h + 1] = f32x4{0.f, 0.f, 0.f, 0.f};
        pin(acc[m][2 * h]); pin(acc[m][2 * h + 1]);
      }
  }
  const u32 fl = WS_FL + pb * FLS + (G == 2 ? 0 : 8) + wx * 4;
  f32x4 gt[2] = {ld4(ea[2] + (G == 2 ? 0 : 768)), ld4(ea[2] + (G == 2 ? 16 : 784))};
  for (int i = 0; i < OWN / 2 - 1; i++) {
    LBAR(); TL(G); SB();
    rd(1, 0, STAGE);
    flip(2 * STAGE, 2 * STAGE);
    mm(0);
    if ((XA & 1) == 0 && i == XA / 2 && ww) {
      WAIT_V(0);
      if (lane == 0) __builtin_amdgcn_raw_buffer_store_b32(1u, rw, fl, 0, 16);
    }
    LBAR(); TL(G); SB();
    rd(0, 0, 0);
    mm(1);
    if ((XA & 1) == 1 && i == XA / 2 && ww) {
      WAIT_V(0);
      if (lane == 0) __builtin_amdgcn_raw_buffer_store_b32(1u, rw, fl, 0, 16);
    }
  }
  LBAR(); TL(G); SB();
  rd(1, 0, STAGE);
  mm(0);
  WAIT_L(); SB();
#pragma unroll
  for (int i = 0; i < 16; i++) {
    const int n = 3 - (i >> 2), m = (n & 1) ? 3 - (i & 3) : (i & 3);
    acc[m][n] = mfma16(fw[1][n], fx[1][m], acc[m][n]);
    if (i >= 4 && i < 12) {
      const int k = i - 4, n = 3 - (k >> 2), m = (n & 1) ? 3 - (k & 3) : (k & 3);
      *(LDS_AS f32x4*)(size_t)(ea[0] + (m * 2 + n - 2) * 1024) = acc[m][n];
    }
    SB();
  }
  LBAR(); TL(G);
  STAMP(G == 2 ? 6 : 18);
  f32x4 pt[4], ex[4][2];
#pragma unroll
  for (int m = 0; m < 4; m++) {
    pt[m] = ld4(ea[1] + m * 1024);
#pragma unroll
    for (int j = 0; j < 2; j++) ex[m][j] = ld4(ea[4] + (m * 2 + j) * 1024);
  }
  SB();
#pragma unroll
  for (int m = 0; m < 4; m++) {
    f32x2 y[4];
#pragma unroll
    for (int i = 0; i < 4; i++) { const float v = pt[m][i]; y[i] = f32x2{bf_lo(f2u(v)), bf_hi(f2u(v))}; }
    SB();
#pragma unroll
    for (int i = 0; i < 4; i++) y[i] += half(acc[m][i >> 1], i & 1);
    SB();
#pragma unroll
    for (int i = 0; i < 4; i++) y[i] += half(ex[m][i >> 1], i & 1);
    SB();
    if (G == 4) {
#pragma unroll
      for (int i = 0; i < 4; i++) {
        const float a = y[i >> 1][i & 1], bb = y[2 + (i >> 1)][i & 1];
        auto sw = __builtin_amdgcn_permlane16_swap(f2u(a), f2u(bb), false, false);
        y[i >> 1][i & 1] = u2f(sw[0]);
        y[2 + (i >> 1)][i & 1] = u2f(sw[1]);
      }
      SB();
    }
#pragma unroll
    for (int i = 0; i < 4; i++) f[m][i] += y[i] * half(gt[i >> 1], i & 1);
    SB();
    if (G == 4) {
      st16(mkrsrc(c.out + m * 32768, S_ * D_ * 2 - m * 32768), ea[3],
           u32x4{pack2(f[m][0][0], f[m][0][1]), pack2(f[m][1][0], f[m][1][1]), pack2(f[m][2][0], f[m][2][1]), pack2(f[m][3][0], f[m][3][1])});
      SB();
    }
  }
  if (G == 4) return;
  float mean[4], m2[4];
  {
    f32x2 t[4][4];
    auto all = [&](auto fn) {
#pragma unroll
      for (int m = 0; m < 4; m++) fn(m);
      SB();
    };
    all([&](int m) { t[m][0] = f[m][0] + f[m][1]; t[m][1] = f[m][2] + f[m][3]; });
    all([&](int m) { t[m][0] += t[m][1]; });
    all([&](int m) { mean[m] = t[m][0][0] + t[m][0][1]; });
    all([&](int m) { mean[m] *= 0.125f; });
    all([&](int m) { for (int i = 0; i < 4; i++) t[m][i] = f[m][i] - mean[m]; });
    all([&](int m) { t[m][0] *= t[m][0]; });
#pragma unroll
    for (int i = 1; i < 4; i++) all([&](int m) { t[m][0] += t[m][i] * t[m][i]; });
    all([&](int m) { m2[m] = t[m][0][0] + t[m][0][1]; });
  }
#pragma unroll
  for (int m = 0; m < 2; m++) {
    auto t = __builtin_amdgcn_permlane32_swap(f2u(mean[m]), f2u(mean[m + 2]), false, false);
    auto u = __builtin_amdgcn_permlane32_swap(f2u(m2[m]), f2u(m2[m + 2]), false, false);
    const float d = u2f(t[0]) - u2f(t[1]);
    mean[m] = 0.5f * (u2f(t[0]) + u2f(t[1]));
    m2[m] = u2f(u[0]) + u2f(u[1]) + d * d * 4.f;
  }
  {
    auto t = __builtin_amdgcn_permlane16_swap(f2u(mean[0]), f2u(mean[1]), false, false);
    auto u = __builtin_amdgcn_permlane16_swap(f2u(m2[0]), f2u(m2[1]), false, false);
    const float d = u2f(t[0]) - u2f(t[1]);
    *(LDS_AS f32x2*)(size_t)ea[3] = f32x2{0.5f * (u2f(t[0]) + u2f(t[1])), u2f(u[0]) + u2f(u[1]) + d * d * 8.f};
  }
  LBAR(); TL(G); SB();
  STAMP(7);
  __builtin_amdgcn_s_sleep(4);
  f32x4 sh[2] = {ld4(ea[2] + 256), ld4(ea[2] + 272)}, sc[2] = {ld4(ea[2] + 512), ld4(ea[2] + 528)};
#pragma unroll
  for (int i = 0; i < 16; i++) {
    const int m = i >> 2, j = i & 3;
    const u32 v = pack2(f[m][j][0], f[m][j][1]);
    x2[m][j] = v;
    f[m][j] = f32x2{bf_lo(v), bf_hi(v)} * half(sc[j >> 1], j & 1);
    pin(f[m][j]);
    if (j == 3) pin(x2[m]);
  }
  u32 px = WS_H2 + (tx * 16 + tw) * 16384 + wx * 8192 + e0 * 32 + cc * 16, sa = AUX + (256 + (wx * 64 + cc) * 2) * 4;
  pin(px); pin(sa); pin(sh[0]); pin(sh[1]);
  LBAR(); TL(G); SB();
  STAMP(8);
  f32x2 st[4];
#pragma unroll
  for (int m = 0; m < 4; m++) st[m] = *(const LDS_AS f32x2*)(size_t)(sa + m * 128);
  SB();
#pragma unroll
  for (int m = 0; m < 4; m++) {
    f32x2 t[4];
#pragma unroll
    for (int i = 0; i < 4; i++) t[i] = half(sc[i >> 1], i & 1) * st[m][0] + half(sh[i >> 1], i & 1);
    SB();
#pragma unroll
    for (int i = 0; i < 4; i++) t[i] += f[m][i] * st[m][1];
    SB();
    __builtin_amdgcn_raw_buffer_store_b128(u32x4{pack2(t[0][0], t[0][1]), pack2(t[1][0], t[1][1]), pack2(t[2][0], t[2][1]), pack2(t[3][0], t[3][1])},
                                           rw, px + (m & 1) * 2048, (m >> 1) * 4096, 16);
    SB();
  }
  publish(c, 1, lane, 32, WS_FL + 2 * FLK + ((tx >> 1) * 32 + lane) * FLS + ((tx & 1) * 16 + tw) * 4);
  STAMP(9);
}

template <int grp>
DI void g3(const Ctx& c, Sk<4>& k4, u32x4* pk, u32& po, Dg& dg) {
  constexpr int N = 16, NT = grp ? 4 : 3, NF = 2 * NT, NM = 4 * NF;
  const T3 t(c);
  const int tm = t.tm, tn = t.tn, lw = t.lw;
  int lane = lane_id(), g = lane >> 4, cc = lane & 15;
  u32 vo[4], wa[4], xa = (lw >> 1) * STAGE + (lw & 1) * 8192 + lane * 16, tab = t.ks(cc);
  vo[0] = lane * 16 + lw * 4096;
#pragma unroll
  for (int i = 0; i < 2 + grp; i++) vo[i + 1] = grp ? t.aoff(i, lane) : t.boff(i, lane);
#pragma unroll
  for (int n = 0; n < 4; n++) {
    const int q4 = cc >> 2, p4 = lane & 3;
    wa[n] = grp ? G3_WB + (g * 8 + q4) * 128 + ((n ^ ((q4 >> 1) | ((g & 1) << 1))) << 5) + p4 * 8
                : G3_WA + ((g >> 1) * 16 + (g & 1) * 4 + q4) * 96 + p4 * 8;
    if (n < 3 + grp) pin(vo[n]);
    if (grp || n == 0) pin(wa[n]);
  }
  pin(xa); pin(tab);
  f32x4 acc[4][NF];
#pragma unroll
  for (int i = 0; i < NM; i++) { acc[i / NF][i % NF] = f32x4{0.f, 0.f, 0.f, 0.f}; pin(acc[i / NF][i % NF]); }
  bf16x8 fx[2][4], fw[2][NF];
  auto ks = [&](int q) { return (u32)__builtin_amdgcn_readlane(tab, q); };
  auto xsrc = [&](int q, int half) { return c.ws + WS_H2 + (size_t)((tm * 2 + half) * N + ks(q)) * 16384; };
  auto rx = [&](int kk, int m, u32 xo) { fx[kk][m] = lds_b128(xo + m * 2048 + kk * 1024); };
  auto rw = [&](int kk, int n, u32 wo) {
    const u32 a = grp ? wa[n & 3] + wo + (n >> 2) * 8192 + kk * 4096 : wo + (n / 3) * 6144 + (n % 3) * 32 + kk * 3072;
    fw[kk][n] = lds_tr2(a, a + (grp ? 512 : 768));
  };
  auto rd = [&](u32 xo, u32 wo, auto piece) {
    sfor<0, 8>([&](auto ic) {
      constexpr int i = decltype(ic)::value, kk = i >> 2, m = i & 3;
      piece(ic);
      rx(kk, m, xo);
      if (2 * m < NF) { rw(kk, 2 * m, wo); rw(kk, 2 * m + 1, wo); }
    });
    SB();
  };
  auto mf = [&](int kk, int i) {
    const int n = i >> 2, m = (n & 1) ? 3 - (i & 3) : (i & 3);
    acc[m][n] = mfma16(fw[kk][n], fx[kk][m], acc[m][n]);
  };
  auto mm = [&](auto a, auto b, auto pre) {
    constexpr int A = decltype(a)::value, B = decltype(b)::value, E = B < 2 * NM ? B : B - 1;
#pragma unroll
    for (int i = A; i < E; i++) mf(i / NM, i % NM);
#pragma unroll
    for (int i = A; i < E; i++) {
      __builtin_amdgcn_sched_group_barrier(0x008, 1, 0);
      __builtin_amdgcn_sched_group_barrier(0x006, 1, 0);
    }
    SB();
    pre();
    SB();
    if (E < B) mf(1, NM - 1);
    SB();
  };
  auto solo = [&](int kk, u32 xo, u32 wo) {
#pragma unroll
    for (int n = 0; n < NF; n++) {
      rw(kk ^ 1, n, wo);
      if (n < 4) rx(kk ^ 1, n, xo);
    }
#pragma unroll
    for (int i = 0; i < NM; i++) mf(kk, i);
#pragma unroll
    for (int i = 0; i < NM; i++) {
      __builtin_amdgcn_sched_group_barrier(0x008, 1, 0);
      __builtin_amdgcn_sched_group_barrier(0x100, 1, 0);
    }
    SB();
    WAIT_L();
    SB();
  };
  const u32 xd = grp * STAGE + lw * 4096;
  const char *p0 = xsrc(0, grp), *p1 = xsrc(1, grp);
  asm volatile("" : "+s"(p0), "+s"(p1));
  u32 s0 = 0, s1 = WO, s2 = 2 * STAGE;
  TL(3);
  if (grp == 0) {
#pragma unroll
    for (int i = 0; i < 2 * NF; i++) rw(i / NF, i % NF, wa[0]);
    SB();
    STAMP(10);
    LBAR();
    glds4_dev(p0, xd, vo[0]);
    if (XB3) BAR();
    glds4_dev(p1, xd + WO, vo[0]);
    p0 = xsrc(2, 0);
    pins(p0);
    WAIT_V(4);
    BAR();
    STAMP(12);
#pragma unroll
    for (int i = 0; i < 8; i++) rx(i >> 2, i & 3, xa);
    SB();
    glds4_dev(p0, xd + 2 * STAGE, vo[0]);
    sfor<NB, 4>([&](auto ic) { glds1<decltype(ic)::value * 1024, 1>(p0 + N * 16384, xd + 3 * STAGE, vo[0]); });
    WAIT_L(); SB();
    const char *px, *py, *pg, *pu;
    u32 xo, wo, wd, xs;
    auto step = [&](int k, auto wv) {
      __builtin_amdgcn_s_setprio(1);
      SB();
      px = xsrc(k + 3, 0); py = px + N * 16384; pg = t.bsrc(c.wg, ks(k + 2)); pu = t.bsrc(c.wu, ks(k + 2));
      wd = t.bdst(k); xs = xd + s0; xo = xa + s1; wo = wa[0] + (~k & 1) * 12288;
      const u32 s = s0; s0 = s1; s1 = s2; s2 = s;
      mm(IC<0>{}, IC<2 * NM>{}, [&]() {
        asm volatile("" : "+s"(px), "+s"(py), "+s"(pg), "+s"(pu), "+s"(wd), "+s"(xs), "+s"(s0), "+s"(s1), "+s"(s2));
        pin(xo); pin(wo);
        WAIT_V(decltype(wv)::value);
        BAR();
      });
      __builtin_amdgcn_s_setprio(0);
    };
    auto reads = [&]() {
      rd(xo, wo, [&](auto ic) {
        constexpr int i = decltype(ic)::value, e = (i + 1) * (4 - NB) / 8;
        if constexpr (i < 4) glds1<(i & 1) * 1024, 0>(i < 2 ? pg : pu, wd + (i >> 1) * 8192, vo[1 + (i & 1)]);
        else glds1<(i & 3) * 1024, 1>(px, xs, vo[0]);
        if constexpr (e > i * (4 - NB) / 8) glds1<(4 - e) * 1024, 1>(py, xs + STAGE, vo[0]);
      });
      WAIT_V(12 - NB);
    };
    step(0, IC<8 - NB>{});
#pragma nounroll
    for (int k = 1; k < N - 3; k++) {
      reads();
      if (k == 3 + 3 * lw) glds_dw(c.wd, AUX + 3328, ((c.b * 4 + lw) * 56 + min(lane_id(), 55)) * 128);
      LBAR(); TL(3);
      step(k, IC<63>{});
    }
    reads();
    sfor<0, NB>([&](auto ic) { glds1<decltype(ic)::value * 1024, 1>(py, xs + STAGE, vo[0]); });
    LBAR(); TL(3);
    __builtin_amdgcn_s_setprio(1);
    SB();
    xo = xa + 2 * STAGE;
    mm(IC<0>{}, IC<NM>{}, [&]() { pin(xo); BAR(); });
    solo(1, xo, wa[0]);
    BAR();
    solo(0, xo, wa[0]);
    WAIT_V(0);
    BAR(); TL(3); SB();
    solo(1, xa, wa[0] + 12288);
    solo(0, xa, wa[0] + 12288);
    mm(IC<NM>{}, IC<2 * NM>{}, []() { if (NPK == 0) BAR(); });
    __builtin_amdgcn_s_setprio(PRA);
  } else {
    const char *px, *pa;
    u32 xo, wd, xs;
    auto prep = [&](int k) {
      px = xsrc(k + 2, 1); pa = t.asrc(c, ks(k + 2));
      wd = t.adst(k); xs = xd + s2; xo = xa + s0;
      const u32 s = s0; s0 = s1; s1 = s2; s2 = s;
    };
    auto keep = [&]() {
      asm volatile("" : "+s"(px), "+s"(pa), "+s"(wd), "+s"(xs), "+s"(s0), "+s"(s1), "+s"(s2));
      pin(xo); pin(wa[0]); pin(wa[1]); pin(wa[2]); pin(wa[3]);
    };
    prep(0);
    keep();
    STAMP(10);
    await1(c, WS_FL + 2 * FLK + (tm * 32 + tn) * FLS + (lane & 31) * 4);
    STAMP(11);
    glds4_dev(p0, xd, vo[0]);
    if (XB3) BAR();
    glds4_dev(p1, xd + WO, vo[0]);
    WAIT_V(4);
    BAR();
    STAMP(12);
    auto flip = [&]() { wa[0] ^= STAGE; wa[1] ^= STAGE; wa[2] ^= STAGE; wa[3] ^= STAGE; };
#pragma nounroll
    for (int k = 1; k < N - 2; k++) {
      rd(xo, 0, [&](auto ic) {
        constexpr int i = decltype(ic)::value;
        if constexpr (i < 3) glds1<i * 1024, 0>(pa, wd, vo[1 + i]);
        else if constexpr (i < 3 + NB) glds1<(i - 3) * 1024, 1>(px, xs, vo[0]);
      });
      WAIT_V(3 + NB);
      LBAR(); TL(3);
      __builtin_amdgcn_s_setprio(1);
      SB();
      flip();
      prep(k);
      mm(IC<0>{}, IC<2 * NM>{}, [&]() { keep(); BAR(); });
      __builtin_amdgcn_s_setprio(0);
    }
    __builtin_amdgcn_s_setprio(PRB);
    glds3(pa, wd, vo[1], vo[2], vo[3]);
    rd(xo, 0, [](auto) {});
    WAIT_V(3);
    BAR();
    flip();
    {
      lane = lane_id();
      const u32 b0 = t.boff(0, lane), b1 = t.boff(1, lane), s = ks(N - 1);
      LBAR(); TL(3);
      glds2(t.bsrc(c.wg, s), t.bdst(N - 1), b0, b1);
      glds2(t.bsrc(c.wu, s), t.bdst(N - 1) + 8192, b0, b1);
      WAIT_V(4);
      BAR();
      k4.init(lane);
      k4.issue<true, 1>(0);
      k4.issue<true, 1>(1);
      WAIT_V(8);
      BAR(); TL(3); SB();
    }
    __builtin_amdgcn_s_setprio(1);
#pragma unroll
    for (int i = 0; i < NM; i++) {
      const int n = i >> 2, m = (n & 1) ? 3 - (i & 3) : (i & 3);
      asm volatile("v_mfma_f32_16x16x32_bf16 %0, %1, %2, %0" : "+v"(acc[m][n]) : "v"(fw[0][n]), "v"(fx[0][m]));
    }
    SB();
#pragma nounroll
    for (int k = 0; k < 2; k++) {
      xa ^= 2 * STAGE;
      pin(xa);
      solo(1, xa, 0);
      solo(0, xa, 0);
      flip();
    }
    lds_inc(774);
    SB();
    mm(IC<NM>{}, IC<2 * NM>{}, []() {});
    __builtin_amdgcn_s_setprio(0);
  }
  TL(3);
  STAMP(13);
  lane = lane_id(); g = lane >> 4; cc = lane & 15;
  const rsrc_t rs = mkrsrc(c.ws);
  const u32 pb = WS_P + (tm * 2 + (lw >> 1)) * 56 * 16384 + (lw & 1) * 8192 + cc * 16;
  auto off = [&](int chunk) { return pb + (chunk >> 3) * 16384 + (chunk & 7) * 256; };
  auto act = [&](auto md, auto at, u32x4& o0, u32x4& o1) {
    const float k1 = -2.302208198f, k3 = -0.1029432397f;
    f32x2 x[8], t[8], p[8];
    auto all = [&](auto f) {
#pragma unroll
      for (int i = 0; i < 8; i++) {
        if constexpr (decltype(md)::value) f(x[i], t[i], p[i]);
        else {
          float a = t[i][0], b = t[i][1], d = p[i][0], e = p[i][1];
          f(x[i][0], a, d); f(x[i][1], b, e);
          asm("" : "+v"(a), "+v"(b), "+v"(d), "+v"(e));
          t[i] = f32x2{a, b}; p[i] = f32x2{d, e};
        }
      }
      SB();
    };
#pragma unroll
    for (int i = 0; i < 8; i++) {
      const int m = at(i >> 1) >> 2, n = at(i >> 1) & 3, j = (i & 1) * 2;
      t[i] = x[i] = f32x2{acc[m][n][j], acc[m][n][j + 1]};
      p[i] = f32x2{acc[m][NT + n][j], acc[m][NT + n][j + 1]};
    }
    all([](auto x, auto& t, auto& p) { t = x * x; });
    all([&](auto x, auto& t, auto& p) { t = t * k3 + k1; });
    all([](auto x, auto& t, auto& p) { t *= x; });
#pragma unroll
    for (int i = 0; i < 16; i++) { const float v = t[i >> 1][i & 1]; t[i >> 1][i & 1] = __builtin_amdgcn_exp2f(v); }
    SB();
    all([](auto x, auto& t, auto& p) { p *= x; t += 1.f; });
#pragma unroll
    for (int i = 0; i < 16; i++) { const float v = t[i >> 1][i & 1]; t[i >> 1][i & 1] = __builtin_amdgcn_rcpf(v); }
    SB();
    all([](auto x, auto& t, auto& p) { p *= t; });
    if (decltype(md)::value == 2) BAR();
    u32 r[8];
#pragma unroll
    for (int i = 0; i < 8; i++) r[i] = pack2(p[i][0], p[i][1]);
    SB();
#pragma unroll
    for (int i = 0; i < 4; i++) {
      const int a = (i >> 1) * 4 + (i & 1);
      auto u = __builtin_amdgcn_permlane16_swap(r[a], r[a + 2], false, false);
      (i < 2 ? o0 : o1)[i & 1] = u[0];
      (i < 2 ? o0 : o1)[2 + (i & 1)] = u[1];
    }
    pin(o0); pin(o1);
  };
  if (grp) {
    po = off(tn * 8 + (g & 1) * 2 + (g >> 1));
    pin(po);
#pragma unroll
    for (int m = 0; m < 4; m++) {
      act(IC<1>{}, [=](int i) { return m * 4 + i; }, pk[2 * m], pk[2 * m + 1]);
#pragma unroll
      for (int i = m * NE / 4; i < (m + 1) * NE / 4; i++) st16(rs, po + i * 1024, pk[i]);
      SB();
    }
  } else {
    const u32 o0 = off(256 + tn * 6 + (g & 1) * 2 + (g >> 1)), o1 = off(256 + tn * 6 + 4 + (g >> 1)) + (g & 1) * 2048;
    sfor<0, 3>([&](auto hc) {
      constexpr int h = decltype(hc)::value;
      u32x4 u, v;
      act(IC<(h < NPK) + (h == NPK - 1)>{}, [=](int i) { return h < 2 ? (h * 2 + (i >> 1)) * 4 + (i & 1) : i * 4 + 2; }, u, v);
      st16(rs, h < 2 ? o0 + h * 4096 : o1, u);
      st16(rs, h < 2 ? o0 + h * 4096 + 2048 : o1 + 4096, v);
      SB();
    });
    STAMP(26);
    k4.init(lane);
    publish(c, 2, lane, 16, WS_FL + FLK + (tm * 32 + lane * 2 + (tn >> 4)) * FLS + 64 + (tn & 15) * 4);
    __builtin_amdgcn_s_setprio(0);
  }
  STAMP(14);
}

__global__ __launch_bounds__(512, 2) __attribute__((amdgpu_num_sgpr(NSG))) void chain(const char* x, const char* wsr, const char* wsi, const char* wdr,
                                                const char* wdi, const char* wg, const char* wu, const char* wd,
                                                const char* cv, const char* wm, char* ws, char* out) {
  extern __shared__ __attribute__((aligned(16))) char smem[];
  Dg dg = {0, 0, 0};
  STAMP(0);
  const int b = blockIdx.x;
  const Ctx c = {x, wsr, wsi, wdr, wdi, wg, wu, wd, cv, wm, ws, out, smem, b, (b >> 3) & 3, b >> 5, b & 7,
                 __builtin_amdgcn_readfirstlane(threadIdx.x >> 6)};
  const u32 l16 = lane_id() * 16;
  glds2(x + (b * 8 + c.w) * 2048, SM + c.w * SMS, l16, l16);
  if (CPF) {
    u64 pc;
    u32 o = ((b >> 3) * 8 + c.w) * 256;
    asm volatile("s_getpc_b64 %0\n.Lcode0:\n\ts_min_u32 %1, %1, .Lcode1-.Lcode0-256" : "=s"(pc), "+s"(o)::"scc");
    stouch2((const char*)(pc & ~127ull) + (o & ~127u));
  }
  if (XBAR) BAR();
  Sk<4> k4(c);
  u32 po = 0;
  if (c.w < 4) {
    u32x4 x2[4];
    k0_mma(c, dg);
    g1_mma(c, x2, dg);
    sk_mma<2>(c, k4, x2, dg);
    g3<0>(c, k4, x2, po, dg);
    sk_mma<4>(c, k4, x2, dg);
  } else {
    Sk<2> k2(c);
    g1_load(c, k2, dg);
    u32x4 pk[8];
    sk_load<2>(c, k2, pk, po, dg);
    g3<1>(c, k4, pk, po, dg);
    sk_load<4>(c, k4, pk, po, dg);
  }
  WAIT_L(); TL(1); TL(2); TL(3); TL(4);
  STAMP_OUT(1, 19);
  asm volatile(".Lcode1:");
}

extern "C" void kernel_launch(void* const* d_in, const int* in_sizes, int n_in, void* d_out, int out_size, void* d_ws,
                              size_t ws_size, hipStream_t stream) {
  const char* x = (const char*)d_in[0];
  char* ws = (char*)d_ws;
  (void)hipFuncSetAttribute((const void*)chain, hipFuncAttributeMaxDynamicSharedMemorySize, LDS_BYTES);
  chain<<<256, 512, LDS_BYTES, stream>>>(x, (const char*)d_in[1], (const char*)d_in[2], (const char*)d_in[3],
                                         (const char*)d_in[4], (const char*)d_in[6], (const char*)d_in[7],
                                         (const char*)d_in[8], (const char*)d_in[9], (const char*)d_in[10], ws, (char*)d_out);
}
```
